```python
import torch

def _softmax(x, axis=-1):
    return torch.softmax(x, dim=axis)

def _gelu(x):
    return torch.nn.functional.gelu(x, approximate='tanh')

def _silu(x):
    return torch.nn.functional.silu(x)

def _sigmoid(x):
    return torch.sigmoid(x)

def _layernorm(x, axis=-1, eps=1e-05):
    m = x.mean(dim=axis, keepdim=True)
    v = x.var(dim=axis, keepdim=True, unbiased=False)
    return (x - m) / torch.sqrt(v + eps)

def _scatter_add(tgt, idx, src, axis=0):
    return tgt.clone().index_add_(axis, idx, src)

def _segment_sum(src, seg, n):
    out = torch.zeros((n,) + src.shape[1:], dtype=src.dtype)
    return out.index_add_(0, seg, src)

def _pad(x, pw):
    flat = tuple((v for pair in reversed(pw) for v in pair))
    return torch.nn.functional.pad(x, flat)
kernel_name = 'sequential_preLN__memory_aug_recurrent_mem_tr__mixer_channel_mlp__prompt'

def setup_inputs():

    _w = lambda a: (lambda t: t.to(torch.bfloat16) if t.is_floating_point() else t)(torch.as_tensor(a))

    def _A_setup():
        B, S, Dm = (1, 2048, 640)
        H, Dh = (32, 192)
        M, seg = (8, 32)
        seg = min(seg, S)
        S = S // seg * seg
        return {'x': _w((lambda *s: torch.randn(*s, dtype=torch.bfloat16) * 0.05)(B, S, Dm)), 'mem0': _w((lambda *s: torch.randn(*s, dtype=torch.bfloat16) * 0.2)(B, M, Dm)), 'Wq': _w((lambda *s: torch.randn(*s, dtype=torch.bfloat16) * 1.6)(Dm, H, Dh) * Dm ** (-0.5)), 'Wk': _w((lambda *s: torch.randn(*s, dtype=torch.bfloat16) * 1.6)(Dm, H, Dh) * Dm ** (-0.5)), 'Wv': _w((lambda *s: torch.randn(*s, dtype=torch.bfloat16) * 0.2)(Dm, H, Dh) * Dm ** (-0.5)), 'Wo': _w((lambda *s: torch.randn(*s, dtype=torch.bfloat16) * 0.2)(H, Dh, Dm) * (H * Dh) ** (-0.5)), 'seg_len': seg}
    _A_in = _A_setup()

    def _B_setup():
        B, S, D, F = (1, 2048, 640, 1536)
        return {'x': _w((lambda *s: torch.randn(*s, dtype=torch.bfloat16) * 0.05)(B, S, D)), 'w1': _w((lambda *s: torch.randn(*s, dtype=torch.bfloat16) * 0.8)(D, F) * D ** (-0.5)), 'b1': _w((lambda *s: torch.randn(*s, dtype=torch.bfloat16) * 0.4)(F)), 'w2': _w((lambda *s: torch.randn(*s, dtype=torch.bfloat16) * 0.05)(F, D) * F ** (-0.5)), 'b2': _w((lambda *s: torch.randn(*s, dtype=torch.bfloat16) * 0.05)(D))}
    _B_in = _B_setup()
    return {'A_x': _A_in['x'], 'A_mem0': _A_in['mem0'], 'A_Wq': _A_in['Wq'], 'A_Wk': _A_in['Wk'], 'A_Wv': _A_in['Wv'], 'A_Wo': _A_in['Wo'], 'A_seg_len': _A_in['seg_len'], 'B_x': _B_in['x'], 'B_w1': _B_in['w1'], 'B_b1': _B_in['b1'], 'B_w2': _B_in['w2'], 'B_b2': _B_in['b2']}

def reference(A_x, A_mem0, A_Wq, A_Wk, A_Wv, A_Wo, A_seg_len, B_x, B_w1, B_b1, B_w2, B_b2):

    def _A_ref(x, mem0, Wq, Wk, Wv, Wo, seg_len):
        B, S, Dm = x.shape
        H, Dh = (Wq.shape[1], Wq.shape[2])
        M = mem0.shape[1]
        scale = Dh ** (-0.5)
        n_seg = S // seg_len
        L = M + seg_len
        idx = torch.arange(L)
        mask = (idx[:, None] < M) | (idx[:, None] >= idx[None, :])
        mem = mem0
        outs = []
        for s in range(n_seg):
            xs = x[:, s * seg_len:(s + 1) * seg_len, :].clone()
            h = torch.concatenate([mem, xs], axis=1)
            q = torch.einsum('btd,dhe->bhte', h, Wq)
            k = torch.einsum('btd,dhe->bhte', h, Wk)
            v = torch.einsum('btd,dhe->bhte', h, Wv)
            scores = torch.einsum('bhqe,bhke->bhqk', q, k) * scale
            scores = torch.where(mask[None, None], scores, -1000000000.0)
            p = _softmax(scores, axis=-1)
            o = torch.einsum('bhqk,bhke->bhqe', p, v)
            o = torch.einsum('bhte,hed->btd', o, Wo)
            mem = o[:, :M, :].clone()
            outs.append(o[:, M:, :])
        return torch.concatenate(outs, axis=1)

    def _B_ref(x, w1, b1, w2, b2):
        y = _layernorm(x, axis=-1)
        h = _gelu(torch.einsum('bsd,df->bsf', y, w1) + b1)
        return torch.einsum('bsf,fd->bsd', h, w2) + b2
    x = A_x
    h = x + _A_ref(_layernorm(x, axis=-1), A_mem0, A_Wq, A_Wk, A_Wv, A_Wo, A_seg_len)
    h2 = h + _B_ref(_layernorm(h, axis=-1), B_w1, B_b1, B_w2, B_b2)
    return h2
```

```cpp
#include <hip/hip_runtime.h>
#include <hip/hip_bf16.h>
#include <stdint.h>

#define S_LEN 2048
#define DM 640
#define NH 32
#define DH 192
#define HD 6144
#define MM 8
#define SEG 32
#define NSEG 64
#define FF 1536

typedef unsigned short u16;
typedef __attribute__((ext_vector_type(8))) short bf8;
typedef __attribute__((ext_vector_type(4))) float f4;
typedef __attribute__((ext_vector_type(4))) unsigned u4;
typedef __attribute__((ext_vector_type(2))) unsigned u2;
typedef __attribute__((ext_vector_type(4))) unsigned short us4;

#define SENT 0xFFFFFFFFu

__device__ __forceinline__ float bf2f(u16 v) { return __builtin_bit_cast(float, (unsigned)v << 16); }
typedef __attribute__((ext_vector_type(2))) __bf16 bfv2;
__device__ __forceinline__ unsigned pack2(float a, float b) {
    bfv2 t = {(__bf16)a, (__bf16)b};
    return __builtin_bit_cast(unsigned, t);
}
__device__ __forceinline__ float rbf(float f) { return __builtin_bit_cast(float, pack2(0.0f, f) & 0xFFFF0000u); }
__device__ __forceinline__ unsigned nosent(unsigned w) { return w == SENT ? 0x7FC07FC0u : w; }

__device__ __forceinline__ f4 mfma16(bf8 a, bf8 b, f4 c) { return __builtin_amdgcn_mfma_f32_16x16x32_bf16(a, b, c, 0, 0, 0); }

__device__ __forceinline__ u2 tr_read(unsigned lds_addr) {
    u2 r;
    asm volatile("ds_read_b64_tr_b16 %0, %1\n\ts_waitcnt lgkmcnt(0)" : "=&v"(r) : "v"(lds_addr) : "memory");
    return r;
}
__device__ __forceinline__ bf8 tr_read2(unsigned a0, unsigned a1) {
    u2 r0, r1;
    asm volatile("ds_read_b64_tr_b16 %0, %2\n\tds_read_b64_tr_b16 %1, %3\n\ts_waitcnt lgkmcnt(0)"
                 : "=&v"(r0), "=&v"(r1) : "v"(a0), "v"(a1) : "memory");
    u4 t = {r0.x, r0.y, r1.x, r1.y};
    return __builtin_bit_cast(bf8, t);
}

template <int NT, int HALF>
__device__ __forceinline__ void tr_read_frags(unsigned a0, bf8 (&f)[NT]) {
    static_assert(NT >= 2 && NT <= 4, "NT");
    u2 r[8];
    if constexpr (NT == 2) {
        asm volatile("ds_read_b64_tr_b16 %0, %4 offset:%5\n\tds_read_b64_tr_b16 %1, %4 offset:%6\n\t"
                     "ds_read_b64_tr_b16 %2, %4 offset:%7\n\tds_read_b64_tr_b16 %3, %4 offset:%8\n\ts_waitcnt lgkmcnt(0)"
                     : "=&v"(r[0]), "=&v"(r[1]), "=&v"(r[2]), "=&v"(r[3])
                     : "v"(a0), "i"(0), "i"(HALF), "i"(32), "i"(32 + HALF) : "memory");
    } else if constexpr (NT == 3) {
        asm volatile("ds_read_b64_tr_b16 %0, %6 offset:%7\n\tds_read_b64_tr_b16 %1, %6 offset:%8\n\t"
                     "ds_read_b64_tr_b16 %2, %6 offset:%9\n\tds_read_b64_tr_b16 %3, %6 offset:%10\n\t"
                     "ds_read_b64_tr_b16 %4, %6 offset:%11\n\tds_read_b64_tr_b16 %5, %6 offset:%12\n\ts_waitcnt lgkmcnt(0)"
                     : "=&v"(r[0]), "=&v"(r[1]), "=&v"(r[2]), "=&v"(r[3]), "=&v"(r[4]), "=&v"(r[5])
                     : "v"(a0), "i"(0), "i"(HALF), "i"(32), "i"(32 + HALF), "i"(64), "i"(64 + HALF) : "memory");
    } else {
        asm volatile("ds_read_b64_tr_b16 %0, %8 offset:%9\n\tds_read_b64_tr_b16 %1, %8 offset:%10\n\t"
                     "ds_read_b64_tr_b16 %2, %8 offset:%11\n\tds_read_b64_tr_b16 %3, %8 offset:%12\n\t"
                     "ds_read_b64_tr_b16 %4, %8 offset:%13\n\tds_read_b64_tr_b16 %5, %8 offset:%14\n\t"
                     "ds_read_b64_tr_b16 %6, %8 offset:%15\n\tds_read_b64_tr_b16 %7, %8 offset:%16\n\ts_waitcnt lgkmcnt(0)"
                     : "=&v"(r[0]), "=&v"(r[1]), "=&v"(r[2]), "=&v"(r[3]), "=&v"(r[4]), "=&v"(r[5]), "=&v"(r[6]), "=&v"(r[7])
                     : "v"(a0), "i"(0), "i"(HALF), "i"(32), "i"(32 + HALF), "i"(64), "i"(64 + HALF), "i"(96), "i"(96 + HALF) : "memory");
    }
#pragma unroll
    for (int i = 0; i < NT; ++i) { u4 t = {r[2 * i].x, r[2 * i].y, r[2 * i + 1].x, r[2 * i + 1].y}; f[i] = __builtin_bit_cast(bf8, t); }
}

__device__ __forceinline__ __amdgpu_buffer_rsrc_t mkrsrc(const void* p, unsigned bytes) {
    return __builtin_amdgcn_make_buffer_rsrc((void*)p, 0, bytes, 0x00020000);
}
__device__ __forceinline__ u4 ld16_sc1(__amdgpu_buffer_rsrc_t r, int off) {
    return __builtin_bit_cast(u4, __builtin_amdgcn_raw_buffer_load_b128(r, off, 0, 16));
}
__device__ __forceinline__ void st16_sc1(__amdgpu_buffer_rsrc_t r, int off, u4 v) {
    __builtin_amdgcn_raw_buffer_store_b128(v, r, off, 0, 16);
}
__device__ __forceinline__ void st8_sc1(__amdgpu_buffer_rsrc_t r, int off, u2 v) {
    __builtin_amdgcn_raw_buffer_store_b64(v, r, off, 0, 16);
}
__device__ __forceinline__ bool ok4(u4 v) { return v.x != SENT && v.y != SENT && v.z != SENT && v.w != SENT; }

#define WS_XN    (0ull)
#define WS_Q     (WS_XN + 2048ull * 640 * 2)
#define WS_K     (WS_Q + 2048ull * 6144 * 2)
#define WS_V     (WS_K + 2048ull * 6144 * 2)
#define WS_O     (WS_V + 2048ull * 6144 * 2)
#define WS_H     (WS_O + 2048ull * 6144 * 2)
#define WS_Y     (WS_H + 2048ull * 640 * 2)
#define WS_HID   (WS_Y + 2048ull * 640 * 2)
#define WS_SENT0 (WS_HID + 2048ull * 1536 * 2)
#define E1_REP   8
#define E1_ONE   (5120ull * 2)
#define E1_STEP  (E1_ONE * E1_REP)
#define E2_STEP  (32ull * 3 * 8 * 192 * 2)
#define E3_STEP  (4ull * 32 * 32 * 40 * 4)
#define WS_E1    (WS_SENT0)
#define WS_E2    (WS_E1 + 65 * E1_STEP)
#define WS_E3    (WS_E2 + 64 * E2_STEP)
#define E3_RING  4
#define WS_SENT1 (WS_E3 + E3_RING * E3_STEP)
#define WS_TMO   (WS_SENT1)
#define WS_FLAG  (WS_TMO + 1024)
#define WS_ZEND  (WS_FLAG + 16 * 32 * 64)

__device__ __forceinline__ float wave_sum(float v) {
#pragma unroll
    for (int o = 32; o >= 1; o >>= 1) v += __shfl_xor(v, o);
    return v;
}
__device__ __forceinline__ void ln_row(float (&x)[10]) {
    float s = 0.f;
#pragma unroll
    for (int i = 0; i < 10; ++i) s += x[i];
    s = wave_sum(s);
    const float mean = s * (1.0f / DM);
    float q = 0.f;
#pragma unroll
    for (int i = 0; i < 10; ++i) { float d = x[i] - mean; q += d * d; }
    q = wave_sum(q);
    const float mb = rbf(mean);
    const float vb = rbf(q * (1.0f / DM));
    const float den = rbf(sqrtf(rbf(vb + 1e-5f)));
#pragma unroll
    for (int i = 0; i < 10; ++i) x[i] = rbf(rbf(x[i] - mb) / den);
}

__global__ void __launch_bounds__(256) ln_kernel(const u16* __restrict__ x, u16* __restrict__ y, int twice) {
    const int row = blockIdx.x * 4 + (threadIdx.x >> 6), lane = threadIdx.x & 63;
    const unsigned* xr = (const unsigned*)(x + (size_t)row * DM);
    float v[10];
#pragma unroll
    for (int i = 0; i < 5; ++i) { unsigned w = xr[i * 64 + lane]; v[2 * i] = bf2f((u16)(w & 0xFFFF)); v[2 * i + 1] = bf2f((u16)(w >> 16)); }
    ln_row(v);
    if (twice) ln_row(v);
    unsigned* yr = (unsigned*)(y + (size_t)row * DM);
#pragma unroll
    for (int i = 0; i < 5; ++i) yr[i * 64 + lane] = pack2(v[2 * i], v[2 * i + 1]);
}

template <int NSPLIT>
__global__ void __launch_bounds__(256) red_ln_kernel(const float* __restrict__ part, const u16* __restrict__ x, u16* __restrict__ hout, u16* __restrict__ y) {
    const int row = blockIdx.x * 4 + (threadIdx.x >> 6), lane = threadIdx.x & 63;
    const unsigned* xr = (const unsigned*)(x + (size_t)row * DM);
    float v[10];
#pragma unroll
    for (int i = 0; i < 5; ++i) {
        float2 a = *(const float2*)(part + (size_t)row * DM + (i * 64 + lane) * 2);
#pragma unroll
        for (int sp = 1; sp < NSPLIT; ++sp) { float2 t = *(const float2*)(part + ((size_t)sp * S_LEN + row) * DM + (i * 64 + lane) * 2); a.x += t.x; a.y += t.y; }
        unsigned w = xr[i * 64 + lane];
        v[2 * i] = rbf(bf2f((u16)(w & 0xFFFF)) + rbf(a.x));
        v[2 * i + 1] = rbf(bf2f((u16)(w >> 16)) + rbf(a.y));
    }
    unsigned* hr = (unsigned*)(hout + (size_t)row * DM);
#pragma unroll
    for (int i = 0; i < 5; ++i) hr[i * 64 + lane] = pack2(v[2 * i], v[2 * i + 1]);
    ln_row(v);
    ln_row(v);
    unsigned* yr = (unsigned*)(y + (size_t)row * DM);
#pragma unroll
    for (int i = 0; i < 5; ++i) yr[i * 64 + lane] = pack2(v[2 * i], v[2 * i + 1]);
}

__device__ __forceinline__ float gelu_tanh(float x) {
    const float k0 = 0.7978845608028654f, k1 = 0.044715f;
    float inner = k0 * (x + k1 * x * x * x);
    return 0.5f * x * (1.0f + tanhf(inner));
}

template <int BM, int BN, int EPI>
__global__ void __launch_bounds__(256) gemm_kernel(const u16* __restrict__ A, int lda,
                                                   const u16* __restrict__ B0, const u16* __restrict__ B1, const u16* __restrict__ B2, int ldb,
                                                   u16* __restrict__ C0, u16* __restrict__ C1, u16* __restrict__ C2, int ldc,
                                                   int K, int nblk_per_mat,
                                                   const u16* __restrict__ aux0, const u16* __restrict__ aux1, float* __restrict__ part) {
    constexpr int BK = 64;
    constexpr int XS = BK * 2 + 16;
    constexpr int WSR = BN * 2 + 32;
    constexpr int XT = BM * XS, WT = BK * WSR;
    constexpr int WM = BM / 2, WN = BN / 2;
    constexpr int TM = WM / 16, TN = WN / 16;
    constexpr int XCH = BM * BK / 8 / 256;
    constexpr int WCH = BK * BN / 8 / 256;
    __shared__ __attribute__((aligned(16))) char smem[2 * (XT + WT)];

    const int tid = threadIdx.x, lane = tid & 63, wave = tid >> 6;
    const int wm = wave >> 1, wn = wave & 1;
    int nb = blockIdx.x, mat = 0;
    if (EPI == 0) { mat = nb / nblk_per_mat; nb -= mat * nblk_per_mat; }
    const u16* B = mat == 0 ? B0 : (mat == 1 ? B1 : B2);
    u16* C = mat == 0 ? C0 : (mat == 1 ? C1 : C2);
    const int m0 = blockIdx.y * BM, n0 = nb * BN;
    if (EPI == 4) { A += (size_t)blockIdx.z * K; B += (size_t)blockIdx.z * K * ldb; }

    u4 xr[XCH], wr[WCH];
    auto gload = [&](int k0) {
#pragma unroll
        for (int i = 0; i < XCH; ++i) {
            int c = tid + i * 256, r = c >> 3, kc = c & 7;
            xr[i] = *(const u4*)(A + (size_t)(m0 + r) * lda + k0 + kc * 8);
        }
#pragma unroll
        for (int i = 0; i < WCH; ++i) {
            int c = tid + i * 256, r = c / (BN / 8), nc = c % (BN / 8);
            wr[i] = *(const u4*)(B + (size_t)(k0 + r) * ldb + n0 + nc * 8);
        }
    };
    auto swrite = [&](int buf) {
        char* xs = smem + buf * (XT + WT); char* ws = xs + XT;
#pragma unroll
        for (int i = 0; i < XCH; ++i) {
            int c = tid + i * 256, r = c >> 3, kc = c & 7;
            *(u4*)(xs + r * XS + kc * 16) = xr[i];
        }
#pragma unroll
        for (int i = 0; i < WCH; ++i) {
            int c = tid + i * 256, r = c / (BN / 8), nc = c % (BN / 8);
            *(u4*)(ws + r * WSR + nc * 16) = wr[i];
        }
    };

    f4 acc[TN][TM];
#pragma unroll
    for (int i = 0; i < TN; ++i)
#pragma unroll
        for (int j = 0; j < TM; ++j) acc[i][j] = (f4){0.f, 0.f, 0.f, 0.f};

    const int g = lane >> 4, li = lane & 15, q = li >> 2, p = li & 3;
    const int nk = K / BK;
    gload(0);
    swrite(0);
    __syncthreads();
    for (int kt = 0; kt < nk; ++kt) {
        const int buf = kt & 1;
        if (kt + 1 < nk) gload((kt + 1) * BK);
        const char* xs = smem + buf * (XT + WT);
        const unsigned wsb = (unsigned)(size_t)(smem + buf * (XT + WT) + XT);
#pragma unroll
        for (int ks = 0; ks < 2; ++ks) {
            bf8 wf[TN], xf[TM];
#pragma unroll
            for (int j = 0; j < TM; ++j)
                xf[j] = *(const bf8*)(xs + (wm * WM + j * 16 + li) * XS + (ks * 32 + 8 * g) * 2);
            tr_read_frags<TN, 4 * WSR>(wsb + (ks * 32 + 8 * g + q) * WSR + (wn * WN + 4 * p) * 2, wf);
#pragma unroll
            for (int i = 0; i < TN; ++i)
#pragma unroll
                for (int j = 0; j < TM; ++j) acc[i][j] = mfma16(wf[i], xf[j], acc[i][j]);
        }
        if (kt + 1 < nk) swrite(buf ^ 1);
        __syncthreads();
    }
#pragma unroll
    for (int i = 0; i < TN; ++i)
#pragma unroll
        for (int j = 0; j < TM; ++j) {
            const int m = m0 + wm * WM + j * 16 + li;
            const int n = n0 + wn * WN + i * 16 + 4 * g;
            if (EPI == 4) { *(f4*)(part + ((size_t)blockIdx.z * S_LEN + m) * ldc + n) = acc[i][j]; continue; }
            float v[4] = {acc[i][j].x, acc[i][j].y, acc[i][j].z, acc[i][j].w};
            if (EPI == 1) {
                us4 xv = *(const us4*)(aux0 + (size_t)m * ldc + n);
#pragma unroll
                for (int e = 0; e < 4; ++e) v[e] = bf2f(xv[e]) + rbf(v[e]);
            } else if (EPI == 2) {
                us4 bv = *(const us4*)(aux0 + n);
#pragma unroll
                for (int e = 0; e < 4; ++e) v[e] = gelu_tanh(rbf(rbf(v[e]) + bf2f(bv[e])));
            } else if (EPI == 3) {
                us4 bv = *(const us4*)(aux0 + n);
                us4 hv = *(const us4*)(aux1 + (size_t)m * ldc + n);
#pragma unroll
                for (int e = 0; e < 4; ++e) v[e] = bf2f(hv[e]) + rbf(rbf(v[e]) + bf2f(bv[e]));
            }
            u2 o = {pack2(v[0], v[1]), pack2(v[2], v[3])};
            *(u2*)(C + (size_t)m * ldc + n) = o;
        }
}

#define KV_BUF  (32 * 384)
#define L_KX    0
#define L_VX    (L_KX + 2 * KV_BUF)
#define L_RED   (L_VX + 2 * KV_BUF)
#define L_RED2  (L_RED + 18432)
#define L_QK    (L_RED2 + 20480)
#define L_STAGE (L_QK + 9600)
#define L_TOTAL (L_STAGE + 51200)
#define SPIN_MAX (1u << 22)
#define DLY1 18
#define DLY2 14
#define DLY3 16
#define DELAY(n) do { for (int d_ = 0; d_ < (n); ++d_) __builtin_amdgcn_s_sleep(1); } while (0)
#define STW(i) do {} while (0)
#define STDUMP() do {} while (0)
#define BAR() asm volatile("s_waitcnt lgkmcnt(0)\n\ts_barrier" ::: "memory")
typedef __attribute__((ext_vector_type(4))) short bf4;

__device__ __forceinline__ f4 mfma16k16(u2 a, u2 b, f4 c) {
    return __builtin_amdgcn_mfma_f32_16x16x16bf16_1k(__builtin_bit_cast(bf4, a), __builtin_bit_cast(bf4, b), c, 0, 0, 0);
}
__device__ __forceinline__ u2 ld8_sc1(__amdgpu_buffer_rsrc_t r, int off) {
    return __builtin_bit_cast(u2, __builtin_amdgcn_raw_buffer_load_b64(r, off, 0, 16));
}
__device__ __forceinline__ float xmax16(float v) {
    unsigned u = __builtin_bit_cast(unsigned, v);
    auto r = __builtin_amdgcn_permlane16_swap(u, u, false, false);
    return fmaxf(__builtin_bit_cast(float, (unsigned)r[0]), __builtin_bit_cast(float, (unsigned)r[1]));
}
__device__ __forceinline__ float xmax32(float v) {
    unsigned u = __builtin_bit_cast(unsigned, v);
    auto r = __builtin_amdgcn_permlane32_swap(u, u, false, false);
    return fmaxf(__builtin_bit_cast(float, (unsigned)r[0]), __builtin_bit_cast(float, (unsigned)r[1]));
}
__device__ __forceinline__ float xsum16(float v) {
    unsigned u = __builtin_bit_cast(unsigned, v);
    auto r = __builtin_amdgcn_permlane16_swap(u, u, false, false);
    return __builtin_bit_cast(float, (unsigned)r[0]) + __builtin_bit_cast(float, (unsigned)r[1]);
}
__device__ __forceinline__ float xsum32(float v) {
    unsigned u = __builtin_bit_cast(unsigned, v);
    auto r = __builtin_amdgcn_permlane32_swap(u, u, false, false);
    return __builtin_bit_cast(float, (unsigned)r[0]) + __builtin_bit_cast(float, (unsigned)r[1]);
}
template <int CTRL>
__device__ __forceinline__ float dpp_add(float v) {
    int t = __builtin_amdgcn_update_dpp(0, __builtin_bit_cast(int, v), CTRL, 0xf, 0xf, true);
    return v + __builtin_bit_cast(float, t);
}
__device__ __forceinline__ float sum32(float v) {
    v = dpp_add<0xB1>(v);
    v = dpp_add<0x4E>(v);
    v = dpp_add<0x141>(v);
    v = dpp_add<0x140>(v);
    return xsum16(v);
}

__device__ __forceinline__ void qkv_gemm_role(char* smem, int pc, const u16* __restrict__ xn,
                                              const u16* __restrict__ Wq, const u16* __restrict__ Wk, const u16* __restrict__ Wv,
                                              u16* Qx, u16* Kx, u16* Vx, char* ws) {
    constexpr int BM = 128, BN = 192, BK = 64;
    constexpr int XS = BK * 2 + 16, WSR = BN * 2 + 32;
    constexpr int XT = BM * XS, WT = BK * WSR;
    const int tid = threadIdx.x, lane = tid & 63, wave = __builtin_amdgcn_readfirstlane(tid >> 6);
    const int wm = wave >> 2, wn = wave & 3;
    const int g = lane >> 4, li = lane & 15, q = li >> 2, p = li & 3;
    for (int item = pc; item < 16 * 96; item += 128) {
        const int rb = item / 96, r96 = item % 96, mi = r96 >> 5, hh = r96 & 31;
        const u16* B = mi == 0 ? Wk : (mi == 1 ? Wv : Wq);
        u16* C = mi == 0 ? Kx : (mi == 1 ? Vx : Qx);
        const int m0 = rb * BM, n0 = hh * DH;
        u4 xr[2], wr[3];
        auto gload = [&](int k0) {
#pragma unroll
            for (int i = 0; i < 2; ++i) { int c = tid + i * 512, r = c >> 3, kc = c & 7; xr[i] = *(const u4*)(xn + (size_t)(m0 + r) * DM + k0 + kc * 8); }
#pragma unroll
            for (int i = 0; i < 3; ++i) { int c = tid + i * 512, r = c / 24, nc = c % 24; wr[i] = *(const u4*)(B + (size_t)(k0 + r) * HD + n0 + nc * 8); }
        };
        auto swrite = [&](int buf) {
            char* xs = smem + buf * (XT + WT); char* wsm = xs + XT;
#pragma unroll
            for (int i = 0; i < 2; ++i) { int c = tid + i * 512, r = c >> 3, kc = c & 7; *(u4*)(xs + r * XS + kc * 16) = xr[i]; }
#pragma unroll
            for (int i = 0; i < 3; ++i) { int c = tid + i * 512, r = c / 24, nc = c % 24; *(u4*)(wsm + r * WSR + nc * 16) = wr[i]; }
        };
        f4 acc[3][4];
#pragma unroll
        for (int i = 0; i < 3; ++i)
#pragma unroll
            for (int jj = 0; jj < 4; ++jj) acc[i][jj] = (f4){0.f, 0.f, 0.f, 0.f};
        gload(0);
        swrite(0);
        __syncthreads();
        for (int kt = 0; kt < DM / BK; ++kt) {
            const int buf = kt & 1;
            if (kt + 1 < DM / BK) gload((kt + 1) * BK);
            const char* xs = smem + buf * (XT + WT);
            const unsigned wsb = (unsigned)(size_t)(smem + buf * (XT + WT) + XT);
#pragma unroll
            for (int ks = 0; ks < 2; ++ks) {
                bf8 wf[3], xf[4];
#pragma unroll
                for (int jj = 0; jj < 4; ++jj) xf[jj] = *(const bf8*)(xs + (wm * 64 + jj * 16 + li) * XS + (ks * 32 + 8 * g) * 2);
                tr_read_frags<3, 4 * WSR>(wsb + (ks * 32 + 8 * g + q) * WSR + (wn * 48 + 4 * p) * 2, wf);
#pragma unroll
                for (int i = 0; i < 3; ++i)
#pragma unroll
                    for (int jj = 0; jj < 4; ++jj) acc[i][jj] = mfma16(wf[i], xf[jj], acc[i][jj]);
            }
            if (kt + 1 < DM / BK) swrite(buf ^ 1);
            __syncthreads();
        }
        __amdgpu_buffer_rsrc_t rc = mkrsrc(C + (size_t)m0 * HD, BM * HD * 2);
#pragma unroll
        for (int i = 0; i < 3; ++i)
#pragma unroll
            for (int jj = 0; jj < 4; ++jj) {
                const int m = wm * 64 + jj * 16 + li, n = n0 + wn * 48 + i * 16 + 4 * g;
                u2 o = {pack2(acc[i][jj].x, acc[i][jj].y), pack2(acc[i][jj].z, acc[i][jj].w)};
                st8_sc1(rc, (m * HD + n) * 2, o);
            }
        asm volatile("s_waitcnt vmcnt(0)" ::: "memory");
        __syncthreads();
        if (tid == 0 && mi < 2) __hip_atomic_fetch_add((unsigned*)(ws + WS_FLAG + (rb * 32 + hh) * 64), 1u, __ATOMIC_RELAXED, __HIP_MEMORY_SCOPE_AGENT);
    }
}

__global__ void __launch_bounds__(512, 1) recur_kernel(const u16* __restrict__ mem0,
                                                       const u16* __restrict__ Wq, const u16* __restrict__ Wk, const u16* __restrict__ Wv,
                                                       const u16* __restrict__ Wo,
                                                       const u16* __restrict__ xn, u16* Qx, u16* Kx, u16* Vx,
                                                       char* __restrict__ ws) {
    extern __shared__ __attribute__((aligned(16))) char smem[];
    const int tid = threadIdx.x, lane = tid & 63, wave = __builtin_amdgcn_readfirstlane(tid >> 6);
    if (blockIdx.x >= 128) { qkv_gemm_role(smem, blockIdx.x - 128, xn, Wq, Wk, Wv, Qx, Kx, Vx, ws); return; }
    const int b = blockIdx.x;
    const int xcd = b & 7, idx = b >> 3;
    const int h = xcd * 4 + (idx >> 2), j = idx & 3;
    const int g = lane >> 4, li = lane & 15, q = li >> 2, p = li & 3;
    const unsigned lbase = (unsigned)(size_t)smem;
    int* tmo = (int*)(ws + WS_TMO);
    const float scale = 0.07216878364870322f;

    auto kv_dma = [&](int s) {
        const int bufo = (s & 1) * KV_BUF;
        if ((s & 3) == 0) {
            const unsigned* fl = (const unsigned*)(ws + WS_FLAG + ((s >> 2) * 32 + h) * 64);
            unsigned spins = 0;
            while (__hip_atomic_load(fl, __ATOMIC_RELAXED, __HIP_MEMORY_SCOPE_AGENT) < 2u) {
                if (++spins > SPIN_MAX) { *tmo = 4; break; }
                __builtin_amdgcn_s_sleep(4);
            }
        }
#pragma unroll 1
        for (int i = 0; i < 12; ++i) {
            const int L = i * 64 + lane, r = L / 24, cp = L % 24, c = (cp & 24) | ((cp ^ r) & 7);
            const size_t goff = (size_t)(s * SEG + r) * HD + h * DH + c * 8;
            __builtin_amdgcn_global_load_lds((const unsigned*)(Kx + goff), (unsigned*)(smem + L_KX + bufo + i * 1024), 16, 0, 16);
            __builtin_amdgcn_global_load_lds((const unsigned*)(Vx + goff), (unsigned*)(smem + L_VX + bufo + i * 1024), 16, 0, 16);
        }
    };

    auto tail = [&](int s) {
        __amdgpu_buffer_rsrc_t r3 = mkrsrc(ws + WS_E3 + (size_t)(s & (E3_RING - 1)) * E3_STEP + (size_t)j * (32 * 32 * 40 * 4), 32 * 32 * 40 * 4);
        if (tid < 320) {
            const int e = tid * 4;
            f4 a = *(const f4*)(smem + L_RED2 + e * 4);
#pragma unroll
            for (int w2 = 1; w2 < 4; ++w2) a += *(const f4*)(smem + L_RED2 + (w2 * 1280 + e) * 4);
            const int dst = e / 40, pos = e % 40;
            u4 o = {nosent(__builtin_bit_cast(unsigned, a.x)), nosent(__builtin_bit_cast(unsigned, a.y)),
                    nosent(__builtin_bit_cast(unsigned, a.z)), nosent(__builtin_bit_cast(unsigned, a.w))};
            st16_sc1(r3, ((dst * 32 + h) * 40 + pos) * 4, o);
        }
        STW(5);
        if (wave >= 3) {
            const int pw = wave - 3, src = lane & 31, part = 2 * pw + (lane >> 5);
            u4 v0;
            unsigned spins = 0;
            DELAY(DLY3);
            for (;;) {
                asm volatile("" ::: "memory");
                v0 = ld16_sc1(r3, ((h * 32 + src) * 40 + part * 4) * 4);
                if (__all(ok4(v0))) break;
                if (++spins > SPIN_MAX) { *tmo = 3; break; }
            }
            STW(6);
            f4 a = __builtin_bit_cast(f4, v0);
            a.x = sum32(a.x); a.y = sum32(a.y); a.z = sum32(a.z); a.w = sum32(a.w);
            __amdgpu_buffer_rsrc_t r1 = mkrsrc(ws + WS_E1 + (size_t)(s + 1) * E1_STEP, (unsigned)E1_STEP);
            const int row = h >> 2, col = 160 * j + (h & 3) * 40 + 8 * pw;
            unsigned w0 = nosent(pack2(a.x, a.y)), w1 = nosent(pack2(a.z, a.w));
            auto s0 = __builtin_amdgcn_permlane32_swap(w0, w0, false, false);
            auto s1 = __builtin_amdgcn_permlane32_swap(w1, w1, false, false);
            u4 o = {(unsigned)s0[0], (unsigned)s1[0], (unsigned)s0[1], (unsigned)s1[1]};
            if (lane < E1_REP) st16_sc1(r1, lane * (int)E1_ONE + (((col >> 5) * 4 + ((col >> 3) & 3)) * 8 + row) * 16, o);
            st16_sc1(r3, ((h * 32 + src) * 40 + part * 4) * 4, (u4){SENT, SENT, SENT, SENT});
            STW(7);
        }
    };

    if (wave < 4) {
        const int kq = wave;
        bf8 wqkv[9][5];
        {
            constexpr int SS = 320;
            for (int kc = 0; kc < 4; ++kc) {
                BAR();
                for (int c = tid; c < 160 * 18; c += 512) {
                    int r = c / 18, cc = c % 18;
                    int gt = 9 * j + (cc >> 1);
                    int m = gt / 12, col = (gt % 12) * 16 + (cc & 1) * 8;
                    const u16* W = m == 0 ? Wq : (m == 1 ? Wk : Wv);
                    u4 v = *(const u4*)(W + (size_t)(160 * kc + r) * HD + h * DH + col);
                    *(u4*)(smem + L_STAGE + r * SS + cc * 16) = v;
                }
                BAR();
                if (kq == kc) {
#pragma unroll
                    for (int t = 0; t < 9; ++t)
#pragma unroll
                        for (int kk = 0; kk < 5; ++kk) {
                            unsigned a0 = lbase + L_STAGE + (kk * 32 + 8 * g + q) * SS + (t * 16 + 4 * p) * 2;
                            wqkv[t][kk] = tr_read2(a0, a0 + 4 * SS);
                        }
                }
            }
            BAR();
            for (int c = tid; c < 192 * 20; c += 512) {
                int r = c / 20, cc = c % 20;
                u4 v = *(const u4*)(Wo + (size_t)(h * DH + r) * DM + 160 * j + cc * 8);
                *(u4*)(smem + L_KX + r * 352 + cc * 16) = v;
            }
            BAR();
            BAR();
        }
        if (wave == 0) kv_dma(0);
        for (int s = 0; s < NSEG; ++s) {
            STW(0);
            bf8 mf[5];
            const int moff = ((li & 7) * DM + (5 * kq) * 32 + 8 * g) * 2;
            const int foff = ((5 * kq * 4 + g) * 8 + (li & 7)) * 16;
            if (s == 0) {
#pragma unroll
                for (int kk = 0; kk < 5; ++kk) mf[kk] = *(const bf8*)((const char*)mem0 + moff + kk * 64);
                asm volatile("s_waitcnt vmcnt(0)" ::: "memory");
            } else {
                __amdgpu_buffer_rsrc_t r = mkrsrc(ws + WS_E1 + (size_t)s * E1_STEP + (size_t)xcd * E1_ONE, (unsigned)E1_ONE);
                unsigned spins = 0;
                DELAY(DLY1);
                for (;;) {
                    asm volatile("" ::: "memory");
                    u4 v[5]; bool ok = true;
#pragma unroll
                    for (int kk = 0; kk < 5; ++kk) v[kk] = ld16_sc1(r, foff + kk * 512);
#pragma unroll
                    for (int kk = 0; kk < 5; ++kk) { ok &= ok4(v[kk]); mf[kk] = __builtin_bit_cast(bf8, v[kk]); }
                    if (__all(ok)) break;
                    if (++spins > SPIN_MAX) { *tmo = 1; break; }
                    __builtin_amdgcn_s_sleep(1);
                }
            }
            STW(1);
#pragma unroll
            for (int tg = 0; tg < 2; ++tg) {
                constexpr int NT0 = 5;
                f4 acc[NT0];
#pragma unroll
                for (int t = 0; t < NT0; ++t) acc[t] = (f4){0.f, 0.f, 0.f, 0.f};
#pragma unroll
                for (int kk = 0; kk < 5; ++kk)
#pragma unroll
                    for (int t = 0; t < NT0; ++t) { const int tt = tg * 5 + t; if (tt < 9) acc[t] = mfma16(wqkv[tt < 9 ? tt : 8][kk], mf[kk], acc[t]); }
                if (li < 8) {
#pragma unroll
                    for (int t = 0; t < NT0; ++t) { const int tt = tg * 5 + t; if (tt < 9) *(f4*)(smem + L_RED + (((kq * 9 + tt) * 8 + li) * 16 + 4 * g) * 4) = acc[t]; }
                }
            }
            STW(2);
            BAR();
            STW(3);
            __amdgpu_buffer_rsrc_t r2 = mkrsrc(ws + WS_E2 + (size_t)s * E2_STEP + (size_t)h * (3 * 8 * 192 * 2), 3 * 8 * 192 * 2);
            if (tid < 144) {
                f4 a0 = *(const f4*)(smem + L_RED + tid * 32), a1 = *(const f4*)(smem + L_RED + tid * 32 + 16);
#pragma unroll
                for (int k2 = 1; k2 < 4; ++k2) {
                    a0 += *(const f4*)(smem + L_RED + k2 * 4608 + tid * 32);
                    a1 += *(const f4*)(smem + L_RED + k2 * 4608 + tid * 32 + 16);
                }
                u4 o = {nosent(pack2(a0.x, a0.y)), nosent(pack2(a0.z, a0.w)), nosent(pack2(a1.x, a1.y)), nosent(pack2(a1.z, a1.w))};
                st16_sc1(r2, j * 2304 + tid * 16, o);
            }
            STW(4);
            if (s == NSEG - 1) break;
            BAR();
            if (wave == 0) kv_dma(s + 1);
            BAR();
            tail(s);
            BAR();
        }
        STDUMP();
    } else {
        const int aw = wave - 4;
        bf8 wo0[10]; u2 wo1[10];
        {
            constexpr int SS = 320;
            for (int kc = 0; kc < 4; ++kc) {
                BAR();
                for (int c = tid; c < 160 * 18; c += 512) {
                    int r = c / 18, cc = c % 18;
                    int gt = 9 * j + (cc >> 1);
                    int m = gt / 12, col = (gt % 12) * 16 + (cc & 1) * 8;
                    const u16* W = m == 0 ? Wq : (m == 1 ? Wk : Wv);
                    u4 v = *(const u4*)(W + (size_t)(160 * kc + r) * HD + h * DH + col);
                    *(u4*)(smem + L_STAGE + r * SS + cc * 16) = v;
                }
                BAR();
            }
            BAR();
            for (int c = tid; c < 192 * 20; c += 512) {
                int r = c / 20, cc = c % 20;
                u4 v = *(const u4*)(Wo + (size_t)(h * DH + r) * DM + 160 * j + cc * 8);
                *(u4*)(smem + L_KX + r * 352 + cc * 16) = v;
            }
            BAR();
#pragma unroll
            for (int ct = 0; ct < 10; ++ct) {
                unsigned a0 = lbase + L_KX + (48 * aw + 4 * g + q) * 352 + (ct * 16 + 4 * p) * 2;
                wo0[ct] = tr_read2(a0, a0 + 16 * 352);
                wo1[ct] = tr_read(a0 + 32 * 352);
            }
            BAR();
        }
        for (int s = 0; s < NSEG; ++s) {
            BAR();
            if (s == NSEG - 1) break;
            const int bufo = (s & 1) * KV_BUF;
            STW(0);
            bf8 kx[2][6], vx[3];
#pragma unroll
            for (int kk = 0; kk < 6; ++kk) {
                const int kc = kk * 4 + g, kcs = ((kc & 24) | ((kc ^ li) & 7)) * 16;
                kx[0][kk] = *(const bf8*)(smem + L_KX + bufo + li * 384 + kcs);
                kx[1][kk] = *(const bf8*)(smem + L_KX + bufo + (16 + li) * 384 + kcs);
            }
#pragma unroll
            for (int dt = 0; dt < 3; ++dt) {
                const int d0 = (3 * aw + dt) * 16;
                const int vr = 4 * g + q, vc = (d0 >> 3) + (p >> 1), vcs = ((vc & 24) | ((vc ^ vr) & 7)) * 16 + (p & 1) * 8;
                unsigned a0 = lbase + L_VX + bufo + vr * 384 + vcs;
                vx[dt] = tr_read2(a0, a0 + 16 * 384);
            }
            STW(1);
            __amdgpu_buffer_rsrc_t r2 = mkrsrc(ws + WS_E2 + (size_t)s * E2_STEP + (size_t)h * (3 * 8 * 192 * 2), 3 * 8 * 192 * 2);
            {
                const int t = tid - 256;
                int cs[3] = {t, t + 256, t + 512 < 576 ? t + 512 : t};
                u4 v[3];
                unsigned spins = 0;
                DELAY(DLY2);
                for (;;) {
                    asm volatile("" ::: "memory");
#pragma unroll
                    for (int i = 0; i < 3; ++i) v[i] = ld16_sc1(r2, cs[i] * 16);
                    if (__all(ok4(v[0]) && ok4(v[1]) && ok4(v[2]))) break;
                    if (++spins > SPIN_MAX) { *tmo = 2; break; }
                }
#pragma unroll
                for (int i = 0; i < 3; ++i) {
                    const int gt = cs[i] >> 4, w = cs[i] & 15;
                    *(u4*)(smem + L_QK + ((gt / 12) * 8 + (w >> 1)) * 400 + ((gt % 12) * 16 + (w & 1) * 8) * 2) = v[i];
                }
            }
            BAR();
            bf8 qf[6], km[6]; u2 vm[3];
#pragma unroll
            for (int kk = 0; kk < 6; ++kk) {
                qf[kk] = *(const bf8*)(smem + L_QK + (li & 7) * 400 + (kk * 32 + 8 * g) * 2);
                km[kk] = *(const bf8*)(smem + L_QK + (8 + (li & 7)) * 400 + (kk * 32 + 8 * g) * 2);
            }
#pragma unroll
            for (int dt = 0; dt < 3; ++dt) vm[dt] = tr_read(lbase + L_QK + (16 + ((4 * g + q) & 7)) * 400 + ((3 * aw + dt) * 16 + 4 * p) * 2);
            STW(2);
            f4 sc[3];
#pragma unroll
            for (int t = 0; t < 3; ++t) sc[t] = (f4){0.f, 0.f, 0.f, 0.f};
#pragma unroll
            for (int kk = 0; kk < 6; ++kk) {
                sc[0] = mfma16(kx[0][kk], qf[kk], sc[0]);
                sc[1] = mfma16(kx[1][kk], qf[kk], sc[1]);
                sc[2] = mfma16(km[kk], qf[kk], sc[2]);
            }
            float sv[12];
#pragma unroll
            for (int t = 0; t < 3; ++t)
#pragma unroll
                for (int e = 0; e < 4; ++e) sv[t * 4 + e] = rbf(rbf(sc[t][e]) * scale);
            if (g >= 2) {
#pragma unroll
                for (int e = 0; e < 4; ++e) sv[8 + e] = -INFINITY;
            }
            float mx = sv[0];
#pragma unroll
            for (int e = 1; e < 12; ++e) mx = fmaxf(mx, sv[e]);
            mx = xmax32(xmax16(mx));
            float sum = 0.f;
#pragma unroll
            for (int e = 0; e < 12; ++e) { sv[e] = __expf(sv[e] - mx); sum += sv[e]; }
            sum = xsum32(xsum16(sum));
            const float inv = 1.0f / sum;
            u4 pb0 = {pack2(sv[0] * inv, sv[1] * inv), pack2(sv[2] * inv, sv[3] * inv), pack2(sv[4] * inv, sv[5] * inv), pack2(sv[6] * inv, sv[7] * inv)};
            u2 pb1 = {pack2(sv[8] * inv, sv[9] * inv), pack2(sv[10] * inv, sv[11] * inv)};
            const bf8 pf0 = __builtin_bit_cast(bf8, pb0);
            f4 oa[3];
#pragma unroll
            for (int dt = 0; dt < 3; ++dt) {
                f4 a = mfma16(vx[dt], pf0, (f4){0.f, 0.f, 0.f, 0.f});
                oa[dt] = mfma16k16(vm[dt], pb1, a);
            }
            u4 ob0 = {pack2(oa[0].x, oa[0].y), pack2(oa[0].z, oa[0].w), pack2(oa[1].x, oa[1].y), pack2(oa[1].z, oa[1].w)};
            u2 ob1 = {pack2(oa[2].x, oa[2].y), pack2(oa[2].z, oa[2].w)};
            const bf8 of0 = __builtin_bit_cast(bf8, ob0);
            f4 pa[10];
#pragma unroll
            for (int ct = 0; ct < 10; ++ct) pa[ct] = mfma16k16(wo1[ct], ob1, mfma16(wo0[ct], of0, (f4){0.f, 0.f, 0.f, 0.f}));
            if (li < 8) {
#pragma unroll
                for (int ct = 0; ct < 10; ++ct) *(f4*)(smem + L_RED2 + ((aw * 8 + li) * 160 + ct * 16 + 4 * g) * 4) = pa[ct];
            }
            STW(3);
            BAR();
            STW(4);
            tail(s);
            BAR();
        }
        STDUMP();
    }
}

#define R_QKV_STRIDE 400
__global__ void __launch_bounds__(128) attn_x_kernel(const u16* __restrict__ Qx, const u16* __restrict__ Kx, const u16* __restrict__ Vx,
                                                     const char* __restrict__ ws, u16* __restrict__ O) {
    __shared__ __attribute__((aligned(16))) char sv[48 * R_QKV_STRIDE];
    const int tid = threadIdx.x, lane = tid & 63, wave = tid >> 6;
    const int h = blockIdx.x & 31, s = blockIdx.x >> 5;
    const int g = lane >> 4, li = lane & 15, q = li >> 2, p = li & 3;
    const u16* e2 = (const u16*)(ws + WS_E2 + (size_t)s * E2_STEP + (size_t)h * (3 * 8 * 192 * 2));
    for (int c = tid; c < 48 * 24; c += 128) {
        int r = c / 24, cc = c % 24;
        u4 v = {0u, 0u, 0u, 0u};
        if (r < 32) v = *(const u4*)(Vx + (size_t)(s * SEG + r) * HD + h * DH + cc * 8);
        else if (r < 40) v = *(const u4*)(e2 + (24 + (cc >> 1)) * 128 + (r - 32) * 16 + (cc & 1) * 8);
        *(u4*)(sv + r * R_QKV_STRIDE + cc * 16) = v;
    }
    const int row = wave * 16 + li;
    f4 sc[3];
#pragma unroll
    for (int t = 0; t < 3; ++t) sc[t] = (f4){0.f, 0.f, 0.f, 0.f};
#pragma unroll
    for (int kk = 0; kk < 6; ++kk) {
        bf8 qf = *(const bf8*)(Qx + (size_t)(s * SEG + row) * HD + h * DH + kk * 32 + 8 * g);
        bf8 k0 = *(const bf8*)(Kx + (size_t)(s * SEG + li) * HD + h * DH + kk * 32 + 8 * g);
        bf8 k1 = *(const bf8*)(Kx + (size_t)(s * SEG + 16 + li) * HD + h * DH + kk * 32 + 8 * g);
        bf8 k2 = *(const bf8*)(e2 + (12 + 2 * kk + (g >> 1)) * 128 + (li & 7) * 16 + (g & 1) * 8);
        sc[0] = mfma16(k0, qf, sc[0]);
        sc[1] = mfma16(k1, qf, sc[1]);
        sc[2] = mfma16(k2, qf, sc[2]);
    }
    const float scale = 0.07216878364870322f;
    float sv2[12];
#pragma unroll
    for (int t = 0; t < 3; ++t)
#pragma unroll
        for (int e = 0; e < 4; ++e) {
            float v = rbf(rbf(sc[t][e]) * scale);
            const int key = t * 16 + 4 * g + e;
            bool valid = t < 2 ? (key <= row) : (g < 2);
            sv2[t * 4 + e] = valid ? v : -INFINITY;
        }
    float mx = sv2[0];
#pragma unroll
    for (int e = 1; e < 12; ++e) mx = fmaxf(mx, sv2[e]);
    mx = fmaxf(mx, __shfl_xor(mx, 16));
    mx = fmaxf(mx, __shfl_xor(mx, 32));
    float sum = 0.f;
#pragma unroll
    for (int e = 0; e < 12; ++e) { sv2[e] = __expf(sv2[e] - mx); sum += sv2[e]; }
    sum += __shfl_xor(sum, 16);
    sum += __shfl_xor(sum, 32);
    const float inv = 1.0f / sum;
    u4 pb0 = {pack2(sv2[0] * inv, sv2[1] * inv), pack2(sv2[2] * inv, sv2[3] * inv), pack2(sv2[4] * inv, sv2[5] * inv), pack2(sv2[6] * inv, sv2[7] * inv)};
    u4 pb1 = {pack2(sv2[8] * inv, sv2[9] * inv), pack2(sv2[10] * inv, sv2[11] * inv), 0u, 0u};
    const bf8 pf0 = __builtin_bit_cast(bf8, pb0), pf1 = __builtin_bit_cast(bf8, pb1);
    __syncthreads();
    const unsigned lbase = (unsigned)(size_t)sv;
#pragma unroll
    for (int dt = 0; dt < 12; ++dt) {
        const int d0 = dt * 16;
        unsigned a0 = lbase + (4 * g + q) * R_QKV_STRIDE + (d0 + 4 * p) * 2;
        bf8 vf0 = tr_read2(a0, a0 + 16 * R_QKV_STRIDE);
        u2 vm = tr_read(a0 + 32 * R_QKV_STRIDE);
        u4 vt = {vm.x, vm.y, 0u, 0u};
        bf8 vf1 = __builtin_bit_cast(bf8, vt);
        f4 a = (f4){0.f, 0.f, 0.f, 0.f};
        a = mfma16(vf0, pf0, a);
        a = mfma16(vf1, pf1, a);
        u2 o = {pack2(a.x, a.y), pack2(a.z, a.w)};
        *(u2*)(O + (size_t)(s * SEG + row) * HD + h * DH + d0 + 4 * g) = o;
    }
}

extern "C" void kernel_launch(void* const* d_in, const int* in_sizes, int n_in,
                              void* d_out, int out_size, void* d_ws, size_t ws_size,
                              hipStream_t stream) {
    const u16* x    = (const u16*)d_in[0];
    const u16* mem0 = (const u16*)d_in[1];
    const u16* Wq   = (const u16*)d_in[2];
    const u16* Wk   = (const u16*)d_in[3];
    const u16* Wv   = (const u16*)d_in[4];
    const u16* Wo   = (const u16*)d_in[5];
    const u16* w1   = (const u16*)d_in[8];
    const u16* b1   = (const u16*)d_in[9];
    const u16* w2   = (const u16*)d_in[10];
    const u16* b2   = (const u16*)d_in[11];
    u16* out = (u16*)d_out;
    char* ws = (char*)d_ws;
    u16* xn = (u16*)(ws + WS_XN);
    u16* Qx = (u16*)(ws + WS_Q);
    u16* Kx = (u16*)(ws + WS_K);
    u16* Vx = (u16*)(ws + WS_V);
    u16* O  = (u16*)(ws + WS_O);
    u16* H  = (u16*)(ws + WS_H);
    u16* Y  = (u16*)(ws + WS_Y);
    u16* HID = (u16*)(ws + WS_HID);

    hipMemsetAsync(ws + WS_SENT0, 0xFF, WS_SENT1 - WS_SENT0, stream);
    hipMemsetAsync(ws + WS_TMO, 0, WS_ZEND - WS_TMO, stream);
    ln_kernel<<<S_LEN / 4, 256, 0, stream>>>(x, xn, 0);
    hipFuncSetAttribute((const void*)recur_kernel, hipFuncAttributeMaxDynamicSharedMemorySize, L_TOTAL);
    recur_kernel<<<256, 512, L_TOTAL, stream>>>(mem0, Wq, Wk, Wv, Wo, xn, Qx, Kx, Vx, ws);
    attn_x_kernel<<<NSEG * NH, 128, 0, stream>>>(Qx, Kx, Vx, ws, O);
    float* PART = (float*)(ws + WS_Q);
    gemm_kernel<128, 128, 4><<<dim3(DM / 128, S_LEN / 128, 4), 256, 0, stream>>>(O, HD, Wo, Wo, Wo, DM, H, H, H, DM, HD / 4, 1, nullptr, nullptr, PART);
    red_ln_kernel<4><<<S_LEN / 4, 256, 0, stream>>>(PART, x, H, Y);
    gemm_kernel<128, 128, 2><<<dim3(FF / 128, S_LEN / 128), 256, 0, stream>>>(Y, DM, w1, w1, w1, FF, HID, HID, HID, FF, DM, 1, b1, nullptr, nullptr);
    gemm_kernel<64, 64, 3><<<dim3(DM / 64, S_LEN / 64), 256, 0, stream>>>(HID, FF, w2, w2, w2, DM, out, out, out, DM, FF, 1, b2, H, nullptr);
}
```

```cpp
#include <hip/hip_runtime.h>
#include <hip/hip_bf16.h>
#include <stdint.h>

#define S_LEN 2048
#define DM 640
#define NH 32
#define DH 192
#define HD 6144
#define MM 8
#define SEG 32
#define NSEG 64
#define FF 1536

typedef unsigned short u16;
typedef __attribute__((ext_vector_type(8))) short bf8;
typedef __attribute__((ext_vector_type(4))) float f4;
typedef __attribute__((ext_vector_type(4))) unsigned u4;
typedef __attribute__((ext_vector_type(2))) unsigned u2;
typedef __attribute__((ext_vector_type(4))) unsigned short us4;

#define SENT 0xFFFFFFFFu

__device__ __forceinline__ float bf2f(u16 v) { return __builtin_bit_cast(float, (unsigned)v << 16); }
typedef __attribute__((ext_vector_type(2))) __bf16 bfv2;
__device__ __forceinline__ unsigned pack2(float a, float b) {
    bfv2 t = {(__bf16)a, (__bf16)b};
    return __builtin_bit_cast(unsigned, t);
}
__device__ __forceinline__ float rbf(float f) { return __builtin_bit_cast(float, pack2(0.0f, f) & 0xFFFF0000u); }
__device__ __forceinline__ unsigned nosent(unsigned w) { return w == SENT ? 0x7FC07FC0u : w; }

__device__ __forceinline__ f4 mfma16(bf8 a, bf8 b, f4 c) { return __builtin_amdgcn_mfma_f32_16x16x32_bf16(a, b, c, 0, 0, 0); }

__device__ __forceinline__ u2 tr_read(unsigned lds_addr) {
    u2 r;
    asm volatile("ds_read_b64_tr_b16 %0, %1\n\ts_waitcnt lgkmcnt(0)" : "=&v"(r) : "v"(lds_addr) : "memory");
    return r;
}
__device__ __forceinline__ bf8 tr_read2(unsigned a0, unsigned a1) {
    u2 r0, r1;
    asm volatile("ds_read_b64_tr_b16 %0, %2\n\tds_read_b64_tr_b16 %1, %3\n\ts_waitcnt lgkmcnt(0)"
                 : "=&v"(r0), "=&v"(r1) : "v"(a0), "v"(a1) : "memory");
    u4 t = {r0.x, r0.y, r1.x, r1.y};
    return __builtin_bit_cast(bf8, t);
}

template <int NT, int HALF>
__device__ __forceinline__ void tr_read_frags(unsigned a0, bf8 (&f)[NT]) {
    static_assert(NT >= 2 && NT <= 4, "NT");
    u2 r[8];
    if constexpr (NT == 2) {
        asm volatile("ds_read_b64_tr_b16 %0, %4 offset:%5\n\tds_read_b64_tr_b16 %1, %4 offset:%6\n\t"
                     "ds_read_b64_tr_b16 %2, %4 offset:%7\n\tds_read_b64_tr_b16 %3, %4 offset:%8\n\ts_waitcnt lgkmcnt(0)"
                     : "=&v"(r[0]), "=&v"(r[1]), "=&v"(r[2]), "=&v"(r[3])
                     : "v"(a0), "i"(0), "i"(HALF), "i"(32), "i"(32 + HALF) : "memory");
    } else if constexpr (NT == 3) {
        asm volatile("ds_read_b64_tr_b16 %0, %6 offset:%7\n\tds_read_b64_tr_b16 %1, %6 offset:%8\n\t"
                     "ds_read_b64_tr_b16 %2, %6 offset:%9\n\tds_read_b64_tr_b16 %3, %6 offset:%10\n\t"
                     "ds_read_b64_tr_b16 %4, %6 offset:%11\n\tds_read_b64_tr_b16 %5, %6 offset:%12\n\ts_waitcnt lgkmcnt(0)"
                     : "=&v"(r[0]), "=&v"(r[1]), "=&v"(r[2]), "=&v"(r[3]), "=&v"(r[4]), "=&v"(r[5])
                     : "v"(a0), "i"(0), "i"(HALF), "i"(32), "i"(32 + HALF), "i"(64), "i"(64 + HALF) : "memory");
    } else {
        asm volatile("ds_read_b64_tr_b16 %0, %8 offset:%9\n\tds_read_b64_tr_b16 %1, %8 offset:%10\n\t"
                     "ds_read_b64_tr_b16 %2, %8 offset:%11\n\tds_read_b64_tr_b16 %3, %8 offset:%12\n\t"
                     "ds_read_b64_tr_b16 %4, %8 offset:%13\n\tds_read_b64_tr_b16 %5, %8 offset:%14\n\t"
                     "ds_read_b64_tr_b16 %6, %8 offset:%15\n\tds_read_b64_tr_b16 %7, %8 offset:%16\n\ts_waitcnt lgkmcnt(0)"
                     : "=&v"(r[0]), "=&v"(r[1]), "=&v"(r[2]), "=&v"(r[3]), "=&v"(r[4]), "=&v"(r[5]), "=&v"(r[6]), "=&v"(r[7])
                     : "v"(a0), "i"(0), "i"(HALF), "i"(32), "i"(32 + HALF), "i"(64), "i"(64 + HALF), "i"(96), "i"(96 + HALF) : "memory");
    }
#pragma unroll
    for (int i = 0; i < NT; ++i) { u4 t = {r[2 * i].x, r[2 * i].y, r[2 * i + 1].x, r[2 * i + 1].y}; f[i] = __builtin_bit_cast(bf8, t); }
}

__device__ __forceinline__ __amdgpu_buffer_rsrc_t mkrsrc(const void* p, unsigned bytes) {
    return __builtin_amdgcn_make_buffer_rsrc((void*)p, 0, bytes, 0x00020000);
}
__device__ __forceinline__ u4 ld16_sc1(__amdgpu_buffer_rsrc_t r, int off) {
    return __builtin_bit_cast(u4, __builtin_amdgcn_raw_buffer_load_b128(r, off, 0, 16));
}
__device__ __forceinline__ void st16_sc1(__amdgpu_buffer_rsrc_t r, int off, u4 v) {
    __builtin_amdgcn_raw_buffer_store_b128(v, r, off, 0, 16);
}
__device__ __forceinline__ void st8_sc1(__amdgpu_buffer_rsrc_t r, int off, u2 v) {
    __builtin_amdgcn_raw_buffer_store_b64(v, r, off, 0, 16);
}
__device__ __forceinline__ bool ok4(u4 v) { return v.x != SENT && v.y != SENT && v.z != SENT && v.w != SENT; }

#define WS_XN    (0ull)
#define WS_Q     (WS_XN + 2048ull * 640 * 2)
#define WS_K     (WS_Q + 2048ull * 6144 * 2)
#define WS_V     (WS_K + 2048ull * 6144 * 2)
#define WS_O     (WS_V + 2048ull * 6144 * 2)
#define WS_H     (WS_O + 2048ull * 6144 * 2)
#define WS_Y     (WS_H + 2048ull * 640 * 2)
#define WS_HID   (WS_Y + 2048ull * 640 * 2)
#define WS_PART  (WS_HID + 2048ull * 1536 * 2)
#define WS_SENT0 (WS_PART + 4ull * 2048 * 640 * 4)
#define E1_REP   8
#define E1_ONE   (5120ull * 2)
#define E1_STEP  (E1_ONE * E1_REP)
#define E2_STEP  (32ull * 3 * 8 * 192 * 2)
#define E3_STEP  (4ull * 32 * 32 * 40 * 4)
#define WS_E1    (WS_SENT0)
#define WS_E2    (WS_E1 + 65 * E1_STEP)
#define WS_E3    (WS_E2 + 64 * E2_STEP)
#define E3_RING  4
#define WS_SENT1 (WS_E3 + E3_RING * E3_STEP)
#define WS_TMO   (WS_SENT1)
#define WS_FLAG  (WS_TMO + 1024)
#define WS_CNTA  (WS_FLAG + 16 * 32 * 64)
#define WS_ZEND  (WS_CNTA + 16 * 64)

__device__ __forceinline__ float wave_sum(float v) {
#pragma unroll
    for (int o = 32; o >= 1; o >>= 1) v += __shfl_xor(v, o);
    return v;
}
__device__ __forceinline__ void ln_row(float (&x)[10]) {
    float s = 0.f;
#pragma unroll
    for (int i = 0; i < 10; ++i) s += x[i];
    s = wave_sum(s);
    const float mean = s * (1.0f / DM);
    float q = 0.f;
#pragma unroll
    for (int i = 0; i < 10; ++i) { float d = x[i] - mean; q += d * d; }
    q = wave_sum(q);
    const float mb = rbf(mean);
    const float vb = rbf(q * (1.0f / DM));
    const float den = rbf(sqrtf(rbf(vb + 1e-5f)));
#pragma unroll
    for (int i = 0; i < 10; ++i) x[i] = rbf(rbf(x[i] - mb) / den);
}

__global__ void __launch_bounds__(256) ln_kernel(const u16* __restrict__ x, u16* __restrict__ y, int twice) {
    const int row = blockIdx.x * 4 + (threadIdx.x >> 6), lane = threadIdx.x & 63;
    const unsigned* xr = (const unsigned*)(x + (size_t)row * DM);
    float v[10];
#pragma unroll
    for (int i = 0; i < 5; ++i) { unsigned w = xr[i * 64 + lane]; v[2 * i] = bf2f((u16)(w & 0xFFFF)); v[2 * i + 1] = bf2f((u16)(w >> 16)); }
    ln_row(v);
    if (twice) ln_row(v);
    unsigned* yr = (unsigned*)(y + (size_t)row * DM);
#pragma unroll
    for (int i = 0; i < 5; ++i) yr[i * 64 + lane] = pack2(v[2 * i], v[2 * i + 1]);
}

__global__ void __launch_bounds__(256) prep_kernel(const u16* __restrict__ x, u16* __restrict__ y, char* __restrict__ ws) {
    if (blockIdx.x < S_LEN / 4) {
        const int row = blockIdx.x * 4 + (threadIdx.x >> 6), lane = threadIdx.x & 63;
        const unsigned* xr = (const unsigned*)(x + (size_t)row * DM);
        float v[10];
#pragma unroll
        for (int i = 0; i < 5; ++i) { unsigned w = xr[i * 64 + lane]; v[2 * i] = bf2f((u16)(w & 0xFFFF)); v[2 * i + 1] = bf2f((u16)(w >> 16)); }
        ln_row(v);
        unsigned* yr = (unsigned*)(y + (size_t)row * DM);
#pragma unroll
        for (int i = 0; i < 5; ++i) yr[i * 64 + lane] = pack2(v[2 * i], v[2 * i + 1]);
    }
    const size_t gt = (size_t)blockIdx.x * 256 + threadIdx.x, nt = (size_t)gridDim.x * 256;
    u4* ps = (u4*)(ws + WS_SENT0);
    const size_t ns = (WS_SENT1 - WS_SENT0) / 16;
    for (size_t i = gt; i < ns; i += nt) ps[i] = (u4){SENT, SENT, SENT, SENT};
    u4* pz = (u4*)(ws + WS_TMO);
    const size_t nz = (WS_ZEND - WS_TMO) / 16;
    for (size_t i = gt; i < nz; i += nt) pz[i] = (u4){0u, 0u, 0u, 0u};
}

template <int NSPLIT>
__global__ void __launch_bounds__(256) red_ln_kernel(const float* __restrict__ part, const u16* __restrict__ x, u16* __restrict__ hout, u16* __restrict__ y) {
    const int row = blockIdx.x * 4 + (threadIdx.x >> 6), lane = threadIdx.x & 63;
    const unsigned* xr = (const unsigned*)(x + (size_t)row * DM);
    float v[10];
#pragma unroll
    for (int i = 0; i < 5; ++i) {
        float2 a = *(const float2*)(part + (size_t)row * DM + (i * 64 + lane) * 2);
#pragma unroll
        for (int sp = 1; sp < NSPLIT; ++sp) { float2 t = *(const float2*)(part + ((size_t)sp * S_LEN + row) * DM + (i * 64 + lane) * 2); a.x += t.x; a.y += t.y; }
        unsigned w = xr[i * 64 + lane];
        v[2 * i] = rbf(bf2f((u16)(w & 0xFFFF)) + rbf(a.x));
        v[2 * i + 1] = rbf(bf2f((u16)(w >> 16)) + rbf(a.y));
    }
    unsigned* hr = (unsigned*)(hout + (size_t)row * DM);
#pragma unroll
    for (int i = 0; i < 5; ++i) hr[i * 64 + lane] = pack2(v[2 * i], v[2 * i + 1]);
    ln_row(v);
    ln_row(v);
    unsigned* yr = (unsigned*)(y + (size_t)row * DM);
#pragma unroll
    for (int i = 0; i < 5; ++i) yr[i * 64 + lane] = pack2(v[2 * i], v[2 * i + 1]);
}

__device__ __forceinline__ float gelu_tanh(float x) {
    const float k0 = 0.7978845608028654f, k1 = 0.044715f;
    float inner = k0 * (x + k1 * x * x * x);
    return 0.5f * x * (1.0f + tanhf(inner));
}

template <int BM, int BN, int EPI>
__global__ void __launch_bounds__(256) gemm_kernel(const u16* __restrict__ A, int lda,
                                                   const u16* __restrict__ B0, const u16* __restrict__ B1, const u16* __restrict__ B2, int ldb,
                                                   u16* __restrict__ C0, u16* __restrict__ C1, u16* __restrict__ C2, int ldc,
                                                   int K, int nblk_per_mat,
                                                   const u16* __restrict__ aux0, const u16* __restrict__ aux1, float* __restrict__ part) {
    constexpr int BK = 64;
    constexpr int XS = BK * 2 + 16;
    constexpr int WSR = BN * 2 + 32;
    constexpr int XT = BM * XS, WT = BK * WSR;
    constexpr int WM = BM / 2, WN = BN / 2;
    constexpr int TM = WM / 16, TN = WN / 16;
    constexpr int XCH = BM * BK / 8 / 256;
    constexpr int WCH = BK * BN / 8 / 256;
    __shared__ __attribute__((aligned(16))) char smem[2 * (XT + WT)];

    const int tid = threadIdx.x, lane = tid & 63, wave = tid >> 6;
    const int wm = wave >> 1, wn = wave & 1;
    int nb = blockIdx.x, mat = 0;
    if (EPI == 0) { mat = nb / nblk_per_mat; nb -= mat * nblk_per_mat; }
    const u16* B = mat == 0 ? B0 : (mat == 1 ? B1 : B2);
    u16* C = mat == 0 ? C0 : (mat == 1 ? C1 : C2);
    const int m0 = blockIdx.y * BM, n0 = nb * BN;
    if (EPI == 4) { A += (size_t)blockIdx.z * K; B += (size_t)blockIdx.z * K * ldb; }

    u4 xr[XCH], wr[WCH];
    auto gload = [&](int k0) {
#pragma unroll
        for (int i = 0; i < XCH; ++i) {
            int c = tid + i * 256, r = c >> 3, kc = c & 7;
            xr[i] = *(const u4*)(A + (size_t)(m0 + r) * lda + k0 + kc * 8);
        }
#pragma unroll
        for (int i = 0; i < WCH; ++i) {
            int c = tid + i * 256, r = c / (BN / 8), nc = c % (BN / 8);
            wr[i] = *(const u4*)(B + (size_t)(k0 + r) * ldb + n0 + nc * 8);
        }
    };
    auto swrite = [&](int buf) {
        char* xs = smem + buf * (XT + WT); char* ws = xs + XT;
#pragma unroll
        for (int i = 0; i < XCH; ++i) {
            int c = tid + i * 256, r = c >> 3, kc = c & 7;
            *(u4*)(xs + r * XS + kc * 16) = xr[i];
        }
#pragma unroll
        for (int i = 0; i < WCH; ++i) {
            int c = tid + i * 256, r = c / (BN / 8), nc = c % (BN / 8);
            *(u4*)(ws + r * WSR + nc * 16) = wr[i];
        }
    };

    f4 acc[TN][TM];
#pragma unroll
    for (int i = 0; i < TN; ++i)
#pragma unroll
        for (int j = 0; j < TM; ++j) acc[i][j] = (f4){0.f, 0.f, 0.f, 0.f};

    const int g = lane >> 4, li = lane & 15, q = li >> 2, p = li & 3;
    const int nk = K / BK;
    gload(0);
    swrite(0);
    __syncthreads();
    for (int kt = 0; kt < nk; ++kt) {
        const int buf = kt & 1;
        if (kt + 1 < nk) gload((kt + 1) * BK);
        const char* xs = smem + buf * (XT + WT);
        const unsigned wsb = (unsigned)(size_t)(smem + buf * (XT + WT) + XT);
#pragma unroll
        for (int ks = 0; ks < 2; ++ks) {
            bf8 wf[TN], xf[TM];
#pragma unroll
            for (int j = 0; j < TM; ++j)
                xf[j] = *(const bf8*)(xs + (wm * WM + j * 16 + li) * XS + (ks * 32 + 8 * g) * 2);
            tr_read_frags<TN, 4 * WSR>(wsb + (ks * 32 + 8 * g + q) * WSR + (wn * WN + 4 * p) * 2, wf);
#pragma unroll
            for (int i = 0; i < TN; ++i)
#pragma unroll
                for (int j = 0; j < TM; ++j) acc[i][j] = mfma16(wf[i], xf[j], acc[i][j]);
        }
        if (kt + 1 < nk) swrite(buf ^ 1);
        __syncthreads();
    }
#pragma unroll
    for (int i = 0; i < TN; ++i)
#pragma unroll
        for (int j = 0; j < TM; ++j) {
            const int m = m0 + wm * WM + j * 16 + li;
            const int n = n0 + wn * WN + i * 16 + 4 * g;
            if (EPI == 4) { *(f4*)(part + ((size_t)blockIdx.z * S_LEN + m) * ldc + n) = acc[i][j]; continue; }
            float v[4] = {acc[i][j].x, acc[i][j].y, acc[i][j].z, acc[i][j].w};
            if (EPI == 1) {
                us4 xv = *(const us4*)(aux0 + (size_t)m * ldc + n);
#pragma unroll
                for (int e = 0; e < 4; ++e) v[e] = bf2f(xv[e]) + rbf(v[e]);
            } else if (EPI == 2) {
                us4 bv = *(const us4*)(aux0 + n);
#pragma unroll
                for (int e = 0; e < 4; ++e) v[e] = gelu_tanh(rbf(rbf(v[e]) + bf2f(bv[e])));
            } else if (EPI == 3) {
                us4 bv = *(const us4*)(aux0 + n);
                us4 hv = *(const us4*)(aux1 + (size_t)m * ldc + n);
#pragma unroll
                for (int e = 0; e < 4; ++e) v[e] = bf2f(hv[e]) + rbf(rbf(v[e]) + bf2f(bv[e]));
            }
            u2 o = {pack2(v[0], v[1]), pack2(v[2], v[3])};
            *(u2*)(C + (size_t)m * ldc + n) = o;
        }
}

#define KV_BUF  (32 * 384)
#define L_KX    0
#define L_VX    (L_KX + 2 * KV_BUF)
#define L_RED   (L_VX + 2 * KV_BUF)
#define L_RED2  (L_RED + 18432)
#define L_QK    (L_RED2 + 20480)
#define L_STAGE (L_QK + 9600)
#define L_TOTAL (L_STAGE + 51200)
#define SPIN_MAX (1u << 22)
#define DLY1 18
#define DLY2 14
#define DLY3 16
#define DELAY(n) do { for (int d_ = 0; d_ < (n); ++d_) __builtin_amdgcn_s_sleep(1); } while (0)
#define ADUP 3
#define ADAPT(d, spins) do {} while (0)
#define STW(i) do {} while (0)
#define STDUMP() do {} while (0)
#define BAR() asm volatile("s_waitcnt lgkmcnt(0)\n\ts_barrier" ::: "memory")
typedef __attribute__((ext_vector_type(4))) short bf4;

__device__ __forceinline__ f4 mfma16k16(u2 a, u2 b, f4 c) {
    u4 a4 = {a.x, a.y, 0u, 0u}, b4 = {b.x, b.y, 0u, 0u};
    return mfma16(__builtin_bit_cast(bf8, a4), __builtin_bit_cast(bf8, b4), c);
}
__device__ __forceinline__ u2 ld8_sc1(__amdgpu_buffer_rsrc_t r, int off) {
    return __builtin_bit_cast(u2, __builtin_amdgcn_raw_buffer_load_b64(r, off, 0, 16));
}
__device__ __forceinline__ float xmax16(float v) {
    unsigned u = __builtin_bit_cast(unsigned, v);
    auto r = __builtin_amdgcn_permlane16_swap(u, u, false, false);
    return fmaxf(__builtin_bit_cast(float, (unsigned)r[0]), __builtin_bit_cast(float, (unsigned)r[1]));
}
__device__ __forceinline__ float xmax32(float v) {
    unsigned u = __builtin_bit_cast(unsigned, v);
    auto r = __builtin_amdgcn_permlane32_swap(u, u, false, false);
    return fmaxf(__builtin_bit_cast(float, (unsigned)r[0]), __builtin_bit_cast(float, (unsigned)r[1]));
}
__device__ __forceinline__ float xsum16(float v) {
    unsigned u = __builtin_bit_cast(unsigned, v);
    auto r = __builtin_amdgcn_permlane16_swap(u, u, false, false);
    return __builtin_bit_cast(float, (unsigned)r[0]) + __builtin_bit_cast(float, (unsigned)r[1]);
}
__device__ __forceinline__ float xsum32(float v) {
    unsigned u = __builtin_bit_cast(unsigned, v);
    auto r = __builtin_amdgcn_permlane32_swap(u, u, false, false);
    return __builtin_bit_cast(float, (unsigned)r[0]) + __builtin_bit_cast(float, (unsigned)r[1]);
}
template <int CTRL>
__device__ __forceinline__ float dpp_add(float v) {
    int t = __builtin_amdgcn_update_dpp(0, __builtin_bit_cast(int, v), CTRL, 0xf, 0xf, true);
    return v + __builtin_bit_cast(float, t);
}
__device__ __forceinline__ float sum32(float v) {
    v = dpp_add<0xB1>(v);
    v = dpp_add<0x4E>(v);
    v = dpp_add<0x141>(v);
    v = dpp_add<0x140>(v);
    return xsum16(v);
}

__device__ __forceinline__ void qkv_gemm_role(char* smem, int pc, const u16* __restrict__ xn,
                                              const u16* __restrict__ Wq, const u16* __restrict__ Wk, const u16* __restrict__ Wv,
                                              const u16* __restrict__ Wo, u16* Qx, u16* Kx, u16* Vx, u16* Og, char* ws) {
    constexpr int BM = 128, BN = 192, BK = 64;
    constexpr int XS = BK * 2 + 16, WSR = BN * 2 + 32;
    constexpr int XT = BM * XS, WT = BK * WSR;
    const int tid = threadIdx.x, lane = tid & 63, wave = __builtin_amdgcn_readfirstlane(tid >> 6);
    const int wm = wave >> 2, wn = wave & 3;
    const int g = lane >> 4, li = lane & 15, q = li >> 2, p = li & 3;
    for (int item = pc; item < 16 * 96; item += 128) {
        const int rb = item / 96, r96 = item % 96, mi = r96 >> 5, hh = r96 & 31;
        const u16* B = mi == 0 ? Wk : (mi == 1 ? Wv : Wq);
        u16* C = mi == 0 ? Kx : (mi == 1 ? Vx : Qx);
        const int m0 = rb * BM, n0 = hh * DH;
        u4 xr[2], wr[3];
        auto gload = [&](int k0) {
#pragma unroll
            for (int i = 0; i < 2; ++i) { int c = tid + i * 512, r = c >> 3, kc = c & 7; xr[i] = *(const u4*)(xn + (size_t)(m0 + r) * DM + k0 + kc * 8); }
#pragma unroll
            for (int i = 0; i < 3; ++i) { int c = tid + i * 512, r = c / 24, nc = c % 24; wr[i] = *(const u4*)(B + (size_t)(k0 + r) * HD + n0 + nc * 8); }
        };
        auto swrite = [&](int buf) {
            char* xs = smem + buf * (XT + WT); char* wsm = xs + XT;
#pragma unroll
            for (int i = 0; i < 2; ++i) { int c = tid + i * 512, r = c >> 3, kc = c & 7; *(u4*)(xs + r * XS + kc * 16) = xr[i]; }
#pragma unroll
            for (int i = 0; i < 3; ++i) { int c = tid + i * 512, r = c / 24, nc = c % 24; *(u4*)(wsm + r * WSR + nc * 16) = wr[i]; }
        };
        f4 acc[3][4];
#pragma unroll
        for (int i = 0; i < 3; ++i)
#pragma unroll
            for (int jj = 0; jj < 4; ++jj) acc[i][jj] = (f4){0.f, 0.f, 0.f, 0.f};
        gload(0);
        swrite(0);
        __syncthreads();
        for (int kt = 0; kt < DM / BK; ++kt) {
            const int buf = kt & 1;
            if (kt + 1 < DM / BK) gload((kt + 1) * BK);
            const char* xs = smem + buf * (XT + WT);
            const unsigned wsb = (unsigned)(size_t)(smem + buf * (XT + WT) + XT);
#pragma unroll
            for (int ks = 0; ks < 2; ++ks) {
                bf8 wf[3], xf[4];
#pragma unroll
                for (int jj = 0; jj < 4; ++jj) xf[jj] = *(const bf8*)(xs + (wm * 64 + jj * 16 + li) * XS + (ks * 32 + 8 * g) * 2);
                tr_read_frags<3, 4 * WSR>(wsb + (ks * 32 + 8 * g + q) * WSR + (wn * 48 + 4 * p) * 2, wf);
#pragma unroll
                for (int i = 0; i < 3; ++i)
#pragma unroll
                    for (int jj = 0; jj < 4; ++jj) acc[i][jj] = mfma16(wf[i], xf[jj], acc[i][jj]);
            }
            if (kt + 1 < DM / BK) swrite(buf ^ 1);
            __syncthreads();
        }
        __amdgpu_buffer_rsrc_t rc = mkrsrc(C + (size_t)m0 * HD, BM * HD * 2);
#pragma unroll
        for (int i = 0; i < 3; ++i)
#pragma unroll
            for (int jj = 0; jj < 4; ++jj) {
                const int m = wm * 64 + jj * 16 + li, n = n0 + wn * 48 + i * 16 + 4 * g;
                u2 o = {pack2(acc[i][jj].x, acc[i][jj].y), pack2(acc[i][jj].z, acc[i][jj].w)};
                st8_sc1(rc, (m * HD + n) * 2, o);
            }
        asm volatile("s_waitcnt vmcnt(0)" ::: "memory");
        __syncthreads();
        if (tid == 0) __hip_atomic_fetch_add((unsigned*)(ws + WS_FLAG + (rb * 32 + hh) * 64 + (mi == 2 ? 32 : 0)), 1u, __ATOMIC_RELAXED, __HIP_MEMORY_SCOPE_AGENT);
    }
    int* tmo = (int*)(ws + WS_TMO);
    const float scale = 0.07216878364870322f;
    for (int item = pc; item < 16 * 52; item += 128) {
        const int rb = item / 52, it = item % 52;
        __syncthreads();
        if (it >= 32) {
            const int nt = (it - 32) % 5, ks = (it - 32) / 5;
            if (tid == 0) {
                const unsigned* fl = (const unsigned*)(ws + WS_CNTA + rb * 64);
                unsigned spins = 0;
                while (__hip_atomic_load(fl, __ATOMIC_RELAXED, __HIP_MEMORY_SCOPE_AGENT) < 32u) {
                    if (++spins > SPIN_MAX) { *tmo = 8; break; }
                    __builtin_amdgcn_s_sleep(16);
                }
                __builtin_amdgcn_fence(__ATOMIC_ACQUIRE, "agent");
                asm volatile("s_waitcnt vmcnt(0)" ::: "memory");
            }
            __syncthreads();
            constexpr int OWSR = 128 * 2 + 32, OWT = BK * OWSR;
            __amdgpu_buffer_rsrc_t ro = mkrsrc(Og + (size_t)rb * 128 * HD, 128 * HD * 2);
            const u16* Bw = Wo + (size_t)ks * 1536 * DM + nt * 128;
            u4 xr[2][2], wr[2][2];
            auto gload = [&](int k0, u4 (&xq)[2], u4 (&wq)[2]) {
#pragma unroll
                for (int i = 0; i < 2; ++i) { int c = tid + i * 512, r = c >> 3, kc = c & 7; xq[i] = *(const u4*)(Og + (size_t)(rb * 128 + r) * HD + ks * 1536 + k0 + kc * 8); }
#pragma unroll
                for (int i = 0; i < 2; ++i) { int c = tid + i * 512, r = c >> 4, nc = c & 15; wq[i] = *(const u4*)(Bw + (size_t)(k0 + r) * DM + nc * 8); }
            };
            auto swrite = [&](int buf, u4 (&xq)[2], u4 (&wq)[2]) {
                char* xs = smem + buf * (XT + OWT); char* wsm = xs + XT;
#pragma unroll
                for (int i = 0; i < 2; ++i) { int c = tid + i * 512, r = c >> 3, kc = c & 7; *(u4*)(xs + r * XS + kc * 16) = xq[i]; }
#pragma unroll
                for (int i = 0; i < 2; ++i) { int c = tid + i * 512, r = c >> 4, nc = c & 15; *(u4*)(wsm + r * OWSR + nc * 16) = wq[i]; }
            };
            f4 acc[2][4];
#pragma unroll
            for (int i = 0; i < 2; ++i)
#pragma unroll
                for (int jj = 0; jj < 4; ++jj) acc[i][jj] = (f4){0.f, 0.f, 0.f, 0.f};
            auto compute = [&](int buf) {
                const char* xs = smem + buf * (XT + OWT);
                const unsigned wsb = (unsigned)(size_t)(smem + buf * (XT + OWT) + XT);
#pragma unroll
                for (int k2 = 0; k2 < 2; ++k2) {
                    bf8 wf[2], xf[4];
#pragma unroll
                    for (int jj = 0; jj < 4; ++jj) xf[jj] = *(const bf8*)(xs + (wm * 64 + jj * 16 + li) * XS + (k2 * 32 + 8 * g) * 2);
                    tr_read_frags<2, 4 * OWSR>(wsb + (k2 * 32 + 8 * g + q) * OWSR + (wn * 32 + 4 * p) * 2, wf);
#pragma unroll
                    for (int i = 0; i < 2; ++i)
#pragma unroll
                        for (int jj = 0; jj < 4; ++jj) acc[i][jj] = mfma16(wf[i], xf[jj], acc[i][jj]);
                }
            };
            gload(0, xr[0], wr[0]);
            gload(BK, xr[1], wr[1]);
            swrite(0, xr[0], wr[0]);
            BAR();
            for (int kt = 0; kt < 24; kt += 2) {
                if (kt + 2 < 24) gload((kt + 2) * BK, xr[0], wr[0]);
                compute(0);
                swrite(1, xr[1], wr[1]);
                BAR();
                if (kt + 3 < 24) gload((kt + 3) * BK, xr[1], wr[1]);
                compute(1);
                if (kt + 2 < 24) swrite(0, xr[0], wr[0]);
                BAR();
            }
            float* part = (float*)(ws + WS_PART);
#pragma unroll
            for (int i = 0; i < 2; ++i)
#pragma unroll
                for (int jj = 0; jj < 4; ++jj) {
                    const int m = rb * 128 + wm * 64 + jj * 16 + li, n = nt * 128 + wn * 32 + i * 16 + 4 * g;
                    *(f4*)(part + ((size_t)ks * S_LEN + m) * DM + n) = acc[i][jj];
                }
            continue;
        }
        const int hh = it;
        if (tid == 0) {
            const unsigned* fl = (const unsigned*)(ws + WS_FLAG + (rb * 32 + hh) * 64);
            unsigned spins = 0;
            while (__hip_atomic_load(fl, __ATOMIC_RELAXED, __HIP_MEMORY_SCOPE_AGENT) < 2u ||
                   __hip_atomic_load(fl + 8, __ATOMIC_RELAXED, __HIP_MEMORY_SCOPE_AGENT) < 1u) {
                if (++spins > SPIN_MAX) { *tmo = 5; break; }
                __builtin_amdgcn_s_sleep(8);
            }
            const unsigned* ew = (const unsigned*)(ws + WS_E2 + (size_t)(rb * 4 + 3) * E2_STEP + (size_t)hh * 9216 + 9212);
            spins = 0;
            while (__hip_atomic_load(ew, __ATOMIC_RELAXED, __HIP_MEMORY_SCOPE_AGENT) == SENT) {
                if (++spins > SPIN_MAX) { *tmo = 7; break; }
                __builtin_amdgcn_s_sleep(48);
            }
        }
        __syncthreads();
        __amdgpu_buffer_rsrc_t rq = mkrsrc(Qx + (size_t)rb * 128 * HD, 128 * HD * 2);
        __amdgpu_buffer_rsrc_t rk = mkrsrc(Kx + (size_t)rb * 128 * HD, 128 * HD * 2);
        __amdgpu_buffer_rsrc_t rv = mkrsrc(Vx + (size_t)rb * 128 * HD, 128 * HD * 2);
        const int sl = wave >> 1, s = rb * 4 + sl;
        __amdgpu_buffer_rsrc_t re = mkrsrc(ws + WS_E2 + (size_t)s * E2_STEP + (size_t)hh * 9216, 9216);
        char* sv = smem + sl * (48 * 400);
        const int t2 = tid & 127;
        for (int c = t2; c < 32 * 24; c += 128) {
            int r = c / 24, cc = c % 24;
            *(u4*)(sv + r * 400 + cc * 16) = ld16_sc1(rv, ((sl * 32 + r) * HD + hh * DH + cc * 8) * 2);
        }
        for (int c = t2; c < 8 * 24; c += 128) *(u4*)(sv + (40 + c / 24) * 400 + (c % 24) * 16) = (u4){0u, 0u, 0u, 0u};
        bf8 km[6];
        {
            const int c0 = t2, c1 = t2 + 128 < 192 ? t2 + 128 : t2;
            const int o0 = ((24 + ((c0 % 24) >> 1)) * 128 + (c0 / 24) * 16 + (c0 & 1) * 8) * 2;
            const int o1 = ((24 + ((c1 % 24) >> 1)) * 128 + (c1 / 24) * 16 + (c1 & 1) * 8) * 2;
            const int ko = ((12 + (g >> 1)) * 128 + (li & 7) * 16 + (g & 1) * 8) * 2;
            unsigned spins = 0;
            u4 v0, v1;
            for (;;) {
                asm volatile("" ::: "memory");
                u4 kk4[6]; bool ok = true;
                v0 = ld16_sc1(re, o0); v1 = ld16_sc1(re, o1);
#pragma unroll
                for (int kk = 0; kk < 6; ++kk) kk4[kk] = ld16_sc1(re, ko + kk * 512);
#pragma unroll
                for (int kk = 0; kk < 6; ++kk) { ok &= ok4(kk4[kk]); km[kk] = __builtin_bit_cast(bf8, kk4[kk]); }
                if (__all(ok && ok4(v0) && ok4(v1))) break;
                if (++spins > SPIN_MAX) { *tmo = 6; break; }
                __builtin_amdgcn_s_sleep(32);
            }
            *(u4*)(sv + (32 + c0 / 24) * 400 + (c0 % 24) * 16) = v0;
            *(u4*)(sv + (32 + c1 / 24) * 400 + (c1 % 24) * 16) = v1;
        }
        const int row = (wave & 1) * 16 + li;
        f4 sc[3];
#pragma unroll
        for (int t = 0; t < 3; ++t) sc[t] = (f4){0.f, 0.f, 0.f, 0.f};
#pragma unroll
        for (int kk = 0; kk < 6; ++kk) {
            bf8 qf = __builtin_bit_cast(bf8, ld16_sc1(rq, ((sl * 32 + row) * HD + hh * DH + kk * 32 + 8 * g) * 2));
            bf8 k0 = __builtin_bit_cast(bf8, ld16_sc1(rk, ((sl * 32 + li) * HD + hh * DH + kk * 32 + 8 * g) * 2));
            bf8 k1 = __builtin_bit_cast(bf8, ld16_sc1(rk, ((sl * 32 + 16 + li) * HD + hh * DH + kk * 32 + 8 * g) * 2));
            sc[0] = mfma16(k0, qf, sc[0]);
            sc[1] = mfma16(k1, qf, sc[1]);
            sc[2] = mfma16(km[kk], qf, sc[2]);
        }
        float sv2[12];
#pragma unroll
        for (int t = 0; t < 3; ++t)
#pragma unroll
            for (int e = 0; e < 4; ++e) {
                float v = rbf(rbf(sc[t][e]) * scale);
                const int key = t * 16 + 4 * g + e;
                bool valid = t < 2 ? (key <= row) : (g < 2);
                sv2[t * 4 + e] = valid ? v : -INFINITY;
            }
        float mx = sv2[0];
#pragma unroll
        for (int e = 1; e < 12; ++e) mx = fmaxf(mx, sv2[e]);
        mx = xmax32(xmax16(mx));
        float sum = 0.f;
#pragma unroll
        for (int e = 0; e < 12; ++e) { sv2[e] = __expf(sv2[e] - mx); sum += sv2[e]; }
        sum = xsum32(xsum16(sum));
        const float inv = 1.0f / sum;
        u4 pb0 = {pack2(sv2[0] * inv, sv2[1] * inv), pack2(sv2[2] * inv, sv2[3] * inv), pack2(sv2[4] * inv, sv2[5] * inv), pack2(sv2[6] * inv, sv2[7] * inv)};
        u2 pb1 = {pack2(sv2[8] * inv, sv2[9] * inv), pack2(sv2[10] * inv, sv2[11] * inv)};
        const bf8 pf0 = __builtin_bit_cast(bf8, pb0);
        __syncthreads();
        const unsigned lb = (unsigned)(size_t)sv;
        __amdgpu_buffer_rsrc_t rog = mkrsrc(Og + (size_t)rb * 128 * HD, 128 * HD * 2);
#pragma unroll
        for (int dt = 0; dt < 12; ++dt) {
            unsigned a0 = lb + (4 * g + q) * 400 + (dt * 16 + 4 * p) * 2;
            bf8 vf0 = tr_read2(a0, a0 + 16 * 400);
            u2 vm = tr_read(a0 + 32 * 400);
            u4 vt = {vm.x, vm.y, 0u, 0u}, pt = {pb1.x, pb1.y, 0u, 0u};
            f4 a = mfma16(__builtin_bit_cast(bf8, vt), __builtin_bit_cast(bf8, pt), mfma16(vf0, pf0, (f4){0.f, 0.f, 0.f, 0.f}));
            u2 o = {pack2(a.x, a.y), pack2(a.z, a.w)};
            st8_sc1(rog, ((sl * 32 + row) * HD + hh * DH + dt * 16 + 4 * g) * 2, o);
        }
        asm volatile("s_waitcnt vmcnt(0)" ::: "memory");
        __syncthreads();
        if (tid == 0) __hip_atomic_fetch_add((unsigned*)(ws + WS_CNTA + rb * 64), 1u, __ATOMIC_RELAXED, __HIP_MEMORY_SCOPE_AGENT);
    }
}

__global__ void __launch_bounds__(512, 1) recur_kernel(const u16* __restrict__ mem0,
                                                       const u16* __restrict__ Wq, const u16* __restrict__ Wk, const u16* __restrict__ Wv,
                                                       const u16* __restrict__ Wo,
                                                       const u16* __restrict__ xn, u16* Qx, u16* Kx, u16* Vx,
                                                       char* __restrict__ ws) {
    extern __shared__ __attribute__((aligned(16))) char smem[];
    const int tid = threadIdx.x, lane = tid & 63, wave = __builtin_amdgcn_readfirstlane(tid >> 6);
    if (blockIdx.x >= 128) { qkv_gemm_role(smem, blockIdx.x - 128, xn, Wq, Wk, Wv, Wo, Qx, Kx, Vx, (u16*)(ws + WS_O), ws);
        return; }
    const int b = blockIdx.x;
    const int xcd = b & 7, idx = b >> 3;
    const int h = xcd * 4 + (idx >> 2), j = idx & 3;
    const int g = lane >> 4, li = lane & 15, q = li >> 2, p = li & 3;
    const unsigned lbase = (unsigned)(size_t)smem;
    int* tmo = (int*)(ws + WS_TMO);
    const float scale = 0.07216878364870322f;
    int dly1 = DLY1, dly2 = DLY2, dly3 = DLY3;

    auto kv_dma = [&](int s) {
        const int bufo = (s & 1) * KV_BUF;
        if ((s & 3) == 0) {
            const unsigned* fl = (const unsigned*)(ws + WS_FLAG + ((s >> 2) * 32 + h) * 64);
            unsigned spins = 0;
            while (__hip_atomic_load(fl, __ATOMIC_RELAXED, __HIP_MEMORY_SCOPE_AGENT) < 2u) {
                if (++spins > SPIN_MAX) { *tmo = 4; break; }
                __builtin_amdgcn_s_sleep(4);
            }
        }
#pragma unroll 1
        for (int i = 0; i < 12; ++i) {
            const int L = i * 64 + lane, r = L / 24, cp = L % 24, c = (cp & 24) | ((cp ^ r) & 7);
            const size_t goff = (size_t)(s * SEG + r) * HD + h * DH + c * 8;
            __builtin_amdgcn_global_load_lds((const unsigned*)(Kx + goff), (unsigned*)(smem + L_KX + bufo + i * 1024), 16, 0, 16);
            __builtin_amdgcn_global_load_lds((const unsigned*)(Vx + goff), (unsigned*)(smem + L_VX + bufo + i * 1024), 16, 0, 16);
        }
    };

    auto tail = [&](int s) {
        __amdgpu_buffer_rsrc_t r3 = mkrsrc(ws + WS_E3 + (size_t)(s & (E3_RING - 1)) * E3_STEP + (size_t)j * (32 * 32 * 40 * 4), 32 * 32 * 40 * 4);
        if (tid < 320) {
            const int e = tid * 4;
            f4 a = *(const f4*)(smem + L_RED2 + e * 4);
#pragma unroll
            for (int w2 = 1; w2 < 4; ++w2) a += *(const f4*)(smem + L_RED2 + (w2 * 1280 + e) * 4);
            const int dst = e / 40, pos = e % 40;
            u4 o = {nosent(__builtin_bit_cast(unsigned, a.x)), nosent(__builtin_bit_cast(unsigned, a.y)),
                    nosent(__builtin_bit_cast(unsigned, a.z)), nosent(__builtin_bit_cast(unsigned, a.w))};
            st16_sc1(r3, ((dst * 32 + h) * 40 + pos) * 4, o);
        }
        STW(5);
        if (wave >= 3) {
            const int pw = wave - 3, src = lane & 31, part = 2 * pw + (lane >> 5);
            u4 v0;
            unsigned spins = 0;
            DELAY(dly3);
            for (;;) {
                asm volatile("" ::: "memory");
                v0 = ld16_sc1(r3, ((h * 32 + src) * 40 + part * 4) * 4);
                if (__all(ok4(v0))) break;
                if (++spins > SPIN_MAX) { *tmo = 3; break; }
            }
            ADAPT(dly3, spins);
            STW(6);
            f4 a = __builtin_bit_cast(f4, v0);
            a.x = sum32(a.x); a.y = sum32(a.y); a.z = sum32(a.z); a.w = sum32(a.w);
            __amdgpu_buffer_rsrc_t r1 = mkrsrc(ws + WS_E1 + (size_t)(s + 1) * E1_STEP, (unsigned)E1_STEP);
            const int row = h >> 2, col = 160 * j + (h & 3) * 40 + 8 * pw;
            unsigned w0 = nosent(pack2(a.x, a.y)), w1 = nosent(pack2(a.z, a.w));
            auto s0 = __builtin_amdgcn_permlane32_swap(w0, w0, false, false);
            auto s1 = __builtin_amdgcn_permlane32_swap(w1, w1, false, false);
            u4 o = {(unsigned)s0[0], (unsigned)s1[0], (unsigned)s0[1], (unsigned)s1[1]};
            if (lane < E1_REP) st16_sc1(r1, lane * (int)E1_ONE + (((col >> 5) * 4 + ((col >> 3) & 3)) * 8 + row) * 16, o);
            st16_sc1(r3, ((h * 32 + src) * 40 + part * 4) * 4, (u4){SENT, SENT, SENT, SENT});
            STW(7);
        }
    };

    if (wave < 4) {
        const int kq = wave;
        bf8 wqkv[9][5];
        {
            constexpr int SS = 320;
            for (int kc = 0; kc < 4; ++kc) {
                BAR();
                for (int c = tid; c < 160 * 18; c += 512) {
                    int r = c / 18, cc = c % 18;
                    int gt = 9 * j + (cc >> 1);
                    int m = gt / 12, col = (gt % 12) * 16 + (cc & 1) * 8;
                    const u16* W = m == 0 ? Wq : (m == 1 ? Wk : Wv);
                    u4 v = *(const u4*)(W + (size_t)(160 * kc + r) * HD + h * DH + col);
                    *(u4*)(smem + L_STAGE + r * SS + cc * 16) = v;
                }
                BAR();
                if (kq == kc) {
#pragma unroll
                    for (int t = 0; t < 9; ++t)
#pragma unroll
                        for (int kk = 0; kk < 5; ++kk) {
                            unsigned a0 = lbase + L_STAGE + (kk * 32 + 8 * g + q) * SS + (t * 16 + 4 * p) * 2;
                            wqkv[t][kk] = tr_read2(a0, a0 + 4 * SS);
                        }
                }
            }
            BAR();
            for (int c = tid; c < 192 * 20; c += 512) {
                int r = c / 20, cc = c % 20;
                u4 v = *(const u4*)(Wo + (size_t)(h * DH + r) * DM + 160 * j + cc * 8);
                *(u4*)(smem + L_KX + r * 352 + cc * 16) = v;
            }
            BAR();
            BAR();
        }
        if (wave == 0) kv_dma(0);
        for (int s = 0; s < NSEG; ++s) {
            STW(0);
            bf8 mf[5];
            const int moff = ((li & 7) * DM + (5 * kq) * 32 + 8 * g) * 2;
            const int foff = ((5 * kq * 4 + g) * 8 + (li & 7)) * 16;
            if (s == 0) {
#pragma unroll
                for (int kk = 0; kk < 5; ++kk) mf[kk] = *(const bf8*)((const char*)mem0 + moff + kk * 64);
                asm volatile("s_waitcnt vmcnt(0)" ::: "memory");
            } else {
                __amdgpu_buffer_rsrc_t r = mkrsrc(ws + WS_E1 + (size_t)s * E1_STEP + (size_t)xcd * E1_ONE, (unsigned)E1_ONE);
                unsigned spins = 0;
                DELAY(dly1);
                for (;;) {
                    asm volatile("" ::: "memory");
                    u4 v[5]; bool ok = true;
#pragma unroll
                    for (int kk = 0; kk < 5; ++kk) v[kk] = ld16_sc1(r, foff + kk * 512);
#pragma unroll
                    for (int kk = 0; kk < 5; ++kk) { ok &= ok4(v[kk]); mf[kk] = __builtin_bit_cast(bf8, v[kk]); }
                    if (__all(ok)) break;
                    if (++spins > SPIN_MAX) { *tmo = 1; break; }
                }
                ADAPT(dly1, spins);
            }
            STW(1);
#pragma unroll
            for (int tg = 0; tg < 2; ++tg) {
                constexpr int NT0 = 5;
                f4 acc[NT0];
#pragma unroll
                for (int t = 0; t < NT0; ++t) acc[t] = (f4){0.f, 0.f, 0.f, 0.f};
#pragma unroll
                for (int kk = 0; kk < 5; ++kk)
#pragma unroll
                    for (int t = 0; t < NT0; ++t) { const int tt = tg * 5 + t; if (tt < 9) acc[t] = mfma16(wqkv[tt < 9 ? tt : 8][kk], mf[kk], acc[t]); }
                if (li < 8) {
#pragma unroll
                    for (int t = 0; t < NT0; ++t) { const int tt = tg * 5 + t; if (tt < 9) *(f4*)(smem + L_RED + (((kq * 9 + tt) * 8 + li) * 16 + 4 * g) * 4) = acc[t]; }
                }
            }
            STW(2);
            BAR();
            STW(3);
            __amdgpu_buffer_rsrc_t r2 = mkrsrc(ws + WS_E2 + (size_t)s * E2_STEP + (size_t)h * (3 * 8 * 192 * 2), 3 * 8 * 192 * 2);
            if (tid < 144) {
                f4 a0 = *(const f4*)(smem + L_RED + tid * 32), a1 = *(const f4*)(smem + L_RED + tid * 32 + 16);
#pragma unroll
                for (int k2 = 1; k2 < 4; ++k2) {
                    a0 += *(const f4*)(smem + L_RED + k2 * 4608 + tid * 32);
                    a1 += *(const f4*)(smem + L_RED + k2 * 4608 + tid * 32 + 16);
                }
                u4 o = {nosent(pack2(a0.x, a0.y)), nosent(pack2(a0.z, a0.w)), nosent(pack2(a1.x, a1.y)), nosent(pack2(a1.z, a1.w))};
                st16_sc1(r2, j * 2304 + tid * 16, o);
            }
            STW(4);
            if (s == NSEG - 1) break;
            BAR();
            if (wave == 0) kv_dma(s + 1);
            BAR();
            tail(s);
            BAR();
        }
        STDUMP();
    } else {
        const int aw = wave - 4;
        bf8 wo0[10]; u2 wo1[10];
        {
            constexpr int SS = 320;
            for (int kc = 0; kc < 4; ++kc) {
                BAR();
                for (int c = tid; c < 160 * 18; c += 512) {
                    int r = c / 18, cc = c % 18;
                    int gt = 9 * j + (cc >> 1);
                    int m = gt / 12, col = (gt % 12) * 16 + (cc & 1) * 8;
                    const u16* W = m == 0 ? Wq : (m == 1 ? Wk : Wv);
                    u4 v = *(const u4*)(W + (size_t)(160 * kc + r) * HD + h * DH + col);
                    *(u4*)(smem + L_STAGE + r * SS + cc * 16) = v;
                }
                BAR();
            }
            BAR();
            for (int c = tid; c < 192 * 20; c += 512) {
                int r = c / 20, cc = c % 20;
                u4 v = *(const u4*)(Wo + (size_t)(h * DH + r) * DM + 160 * j + cc * 8);
                *(u4*)(smem + L_KX + r * 352 + cc * 16) = v;
            }
            BAR();
#pragma unroll
            for (int ct = 0; ct < 10; ++ct) {
                unsigned a0 = lbase + L_KX + (48 * aw + 4 * g + q) * 352 + (ct * 16 + 4 * p) * 2;
                wo0[ct] = tr_read2(a0, a0 + 16 * 352);
                wo1[ct] = tr_read(a0 + 32 * 352);
            }
            BAR();
        }
        for (int s = 0; s < NSEG; ++s) {
            BAR();
            if (s == NSEG - 1) break;
            const int bufo = (s & 1) * KV_BUF;
            STW(0);
            bf8 kx[2][6], vx[3];
#pragma unroll
            for (int kk = 0; kk < 6; ++kk) {
                const int kc = kk * 4 + g, kcs = ((kc & 24) | ((kc ^ li) & 7)) * 16;
                kx[0][kk] = *(const bf8*)(smem + L_KX + bufo + li * 384 + kcs);
                kx[1][kk] = *(const bf8*)(smem + L_KX + bufo + (16 + li) * 384 + kcs);
            }
#pragma unroll
            for (int dt = 0; dt < 3; ++dt) {
                const int d0 = (3 * aw + dt) * 16;
                const int vr = 4 * g + q, vc = (d0 >> 3) + (p >> 1), vcs = ((vc & 24) | ((vc ^ vr) & 7)) * 16 + (p & 1) * 8;
                unsigned a0 = lbase + L_VX + bufo + vr * 384 + vcs;
                vx[dt] = tr_read2(a0, a0 + 16 * 384);
            }
            STW(1);
            __amdgpu_buffer_rsrc_t r2 = mkrsrc(ws + WS_E2 + (size_t)s * E2_STEP + (size_t)h * (3 * 8 * 192 * 2), 3 * 8 * 192 * 2);
            {
                const int t = tid - 256;
                int cs[3] = {t, t + 256, t + 512 < 576 ? t + 512 : t};
                u4 v[3];
                unsigned spins = 0;
                DELAY(dly2);
                for (;;) {
                    asm volatile("" ::: "memory");
#pragma unroll
                    for (int i = 0; i < 3; ++i) v[i] = ld16_sc1(r2, cs[i] * 16);
                    if (__all(ok4(v[0]) && ok4(v[1]) && ok4(v[2]))) break;
                    if (++spins > SPIN_MAX) { *tmo = 2; break; }
                }
                ADAPT(dly2, spins);
#pragma unroll
                for (int i = 0; i < 3; ++i) {
                    const int gt = cs[i] >> 4, w = cs[i] & 15;
                    *(u4*)(smem + L_QK + ((gt / 12) * 8 + (w >> 1)) * 400 + ((gt % 12) * 16 + (w & 1) * 8) * 2) = v[i];
                }
            }
            BAR();
            bf8 qf[6], km[6]; u2 vm[3];
#pragma unroll
            for (int kk = 0; kk < 6; ++kk) {
                qf[kk] = *(const bf8*)(smem + L_QK + (li & 7) * 400 + (kk * 32 + 8 * g) * 2);
                km[kk] = *(const bf8*)(smem + L_QK + (8 + (li & 7)) * 400 + (kk * 32 + 8 * g) * 2);
            }
#pragma unroll
            for (int dt = 0; dt < 3; ++dt) vm[dt] = tr_read(lbase + L_QK + (16 + ((4 * g + q) & 7)) * 400 + ((3 * aw + dt) * 16 + 4 * p) * 2);
            STW(2);
            f4 sc[3];
#pragma unroll
            for (int t = 0; t < 3; ++t) sc[t] = (f4){0.f, 0.f, 0.f, 0.f};
#pragma unroll
            for (int kk = 0; kk < 6; ++kk) {
                sc[0] = mfma16(kx[0][kk], qf[kk], sc[0]);
                sc[1] = mfma16(kx[1][kk], qf[kk], sc[1]);
                sc[2] = mfma16(km[kk], qf[kk], sc[2]);
            }
            float sv[12];
#pragma unroll
            for (int t = 0; t < 3; ++t)
#pragma unroll
                for (int e = 0; e < 4; ++e) sv[t * 4 + e] = rbf(rbf(sc[t][e]) * scale);
            if (g >= 2) {
#pragma unroll
                for (int e = 0; e < 4; ++e) sv[8 + e] = -INFINITY;
            }
            float mx = sv[0];
#pragma unroll
            for (int e = 1; e < 12; ++e) mx = fmaxf(mx, sv[e]);
            mx = xmax32(xmax16(mx));
            float sum = 0.f;
#pragma unroll
            for (int e = 0; e < 12; ++e) { sv[e] = __expf(sv[e] - mx); sum += sv[e]; }
            sum = xsum32(xsum16(sum));
            const float inv = 1.0f / sum;
            u4 pb0 = {pack2(sv[0] * inv, sv[1] * inv), pack2(sv[2] * inv, sv[3] * inv), pack2(sv[4] * inv, sv[5] * inv), pack2(sv[6] * inv, sv[7] * inv)};
            u2 pb1 = {pack2(sv[8] * inv, sv[9] * inv), pack2(sv[10] * inv, sv[11] * inv)};
            const bf8 pf0 = __builtin_bit_cast(bf8, pb0);
            f4 oa[3];
#pragma unroll
            for (int dt = 0; dt < 3; ++dt) {
                f4 a = mfma16(vx[dt], pf0, (f4){0.f, 0.f, 0.f, 0.f});
                oa[dt] = mfma16k16(vm[dt], pb1, a);
            }
            u4 ob0 = {pack2(oa[0].x, oa[0].y), pack2(oa[0].z, oa[0].w), pack2(oa[1].x, oa[1].y), pack2(oa[1].z, oa[1].w)};
            u2 ob1 = {pack2(oa[2].x, oa[2].y), pack2(oa[2].z, oa[2].w)};
            const bf8 of0 = __builtin_bit_cast(bf8, ob0);
            f4 pa[10];
#pragma unroll
            for (int ct = 0; ct < 10; ++ct) pa[ct] = mfma16k16(wo1[ct], ob1, mfma16(wo0[ct], of0, (f4){0.f, 0.f, 0.f, 0.f}));
            if (li < 8) {
#pragma unroll
                for (int ct = 0; ct < 10; ++ct) *(f4*)(smem + L_RED2 + ((aw * 8 + li) * 160 + ct * 16 + 4 * g) * 4) = pa[ct];
            }
            STW(3);
            BAR();
            STW(4);
            tail(s);
            BAR();
        }
        STDUMP();
    }
}

#define R_QKV_STRIDE 400
__global__ void __launch_bounds__(128) attn_x_kernel(const u16* __restrict__ Qx, const u16* __restrict__ Kx, const u16* __restrict__ Vx,
                                                     const char* __restrict__ ws, u16* __restrict__ O) {
    __shared__ __attribute__((aligned(16))) char sv[48 * R_QKV_STRIDE];
    const int tid = threadIdx.x, lane = tid & 63, wave = tid >> 6;
    const int h = blockIdx.x & 31, s = blockIdx.x >> 5;
    const int g = lane >> 4, li = lane & 15, q = li >> 2, p = li & 3;
    const u16* e2 = (const u16*)(ws + WS_E2 + (size_t)s * E2_STEP + (size_t)h * (3 * 8 * 192 * 2));
    for (int c = tid; c < 48 * 24; c += 128) {
        int r = c / 24, cc = c % 24;
        u4 v = {0u, 0u, 0u, 0u};
        if (r < 32) v = *(const u4*)(Vx + (size_t)(s * SEG + r) * HD + h * DH + cc * 8);
        else if (r < 40) v = *(const u4*)(e2 + (24 + (cc >> 1)) * 128 + (r - 32) * 16 + (cc & 1) * 8);
        *(u4*)(sv + r * R_QKV_STRIDE + cc * 16) = v;
    }
    const int row = wave * 16 + li;
    f4 sc[3];
#pragma unroll
    for (int t = 0; t < 3; ++t) sc[t] = (f4){0.f, 0.f, 0.f, 0.f};
#pragma unroll
    for (int kk = 0; kk < 6; ++kk) {
        bf8 qf = *(const bf8*)(Qx + (size_t)(s * SEG + row) * HD + h * DH + kk * 32 + 8 * g);
        bf8 k0 = *(const bf8*)(Kx + (size_t)(s * SEG + li) * HD + h * DH + kk * 32 + 8 * g);
        bf8 k1 = *(const bf8*)(Kx + (size_t)(s * SEG + 16 + li) * HD + h * DH + kk * 32 + 8 * g);
        bf8 k2 = *(const bf8*)(e2 + (12 + 2 * kk + (g >> 1)) * 128 + (li & 7) * 16 + (g & 1) * 8);
        sc[0] = mfma16(k0, qf, sc[0]);
        sc[1] = mfma16(k1, qf, sc[1]);
        sc[2] = mfma16(k2, qf, sc[2]);
    }
    const float scale = 0.07216878364870322f;
    float sv2[12];
#pragma unroll
    for (int t = 0; t < 3; ++t)
#pragma unroll
        for (int e = 0; e < 4; ++e) {
            float v = rbf(rbf(sc[t][e]) * scale);
            const int key = t * 16 + 4 * g + e;
            bool valid = t < 2 ? (key <= row) : (g < 2);
            sv2[t * 4 + e] = valid ? v : -INFINITY;
        }
    float mx = sv2[0];
#pragma unroll
    for (int e = 1; e < 12; ++e) mx = fmaxf(mx, sv2[e]);
    mx = fmaxf(mx, __shfl_xor(mx, 16));
    mx = fmaxf(mx, __shfl_xor(mx, 32));
    float sum = 0.f;
#pragma unroll
    for (int e = 0; e < 12; ++e) { sv2[e] = __expf(sv2[e] - mx); sum += sv2[e]; }
    sum += __shfl_xor(sum, 16);
    sum += __shfl_xor(sum, 32);
    const float inv = 1.0f / sum;
    u4 pb0 = {pack2(sv2[0] * inv, sv2[1] * inv), pack2(sv2[2] * inv, sv2[3] * inv), pack2(sv2[4] * inv, sv2[5] * inv), pack2(sv2[6] * inv, sv2[7] * inv)};
    u4 pb1 = {pack2(sv2[8] * inv, sv2[9] * inv), pack2(sv2[10] * inv, sv2[11] * inv), 0u, 0u};
    const bf8 pf0 = __builtin_bit_cast(bf8, pb0), pf1 = __builtin_bit_cast(bf8, pb1);
    __syncthreads();
    const unsigned lbase = (unsigned)(size_t)sv;
#pragma unroll
    for (int dt = 0; dt < 12; ++dt) {
        const int d0 = dt * 16;
        unsigned a0 = lbase + (4 * g + q) * R_QKV_STRIDE + (d0 + 4 * p) * 2;
        bf8 vf0 = tr_read2(a0, a0 + 16 * R_QKV_STRIDE);
        u2 vm = tr_read(a0 + 32 * R_QKV_STRIDE);
        u4 vt = {vm.x, vm.y, 0u, 0u};
        bf8 vf1 = __builtin_bit_cast(bf8, vt);
        f4 a = (f4){0.f, 0.f, 0.f, 0.f};
        a = mfma16(vf0, pf0, a);
        a = mfma16(vf1, pf1, a);
        u2 o = {pack2(a.x, a.y), pack2(a.z, a.w)};
        *(u2*)(O + (size_t)(s * SEG + row) * HD + h * DH + d0 + 4 * g) = o;
    }
}

extern "C" void kernel_launch(void* const* d_in, const int* in_sizes, int n_in,
                              void* d_out, int out_size, void* d_ws, size_t ws_size,
                              hipStream_t stream) {
    const u16* x    = (const u16*)d_in[0];
    const u16* mem0 = (const u16*)d_in[1];
    const u16* Wq   = (const u16*)d_in[2];
    const u16* Wk   = (const u16*)d_in[3];
    const u16* Wv   = (const u16*)d_in[4];
    const u16* Wo   = (const u16*)d_in[5];
    const u16* w1   = (const u16*)d_in[8];
    const u16* b1   = (const u16*)d_in[9];
    const u16* w2   = (const u16*)d_in[10];
    const u16* b2   = (const u16*)d_in[11];
    u16* out = (u16*)d_out;
    char* ws = (char*)d_ws;
    u16* xn = (u16*)(ws + WS_XN);
    u16* Qx = (u16*)(ws + WS_Q);
    u16* Kx = (u16*)(ws + WS_K);
    u16* Vx = (u16*)(ws + WS_V);
    u16* O  = (u16*)(ws + WS_O);
    u16* H  = (u16*)(ws + WS_H);
    u16* Y  = (u16*)(ws + WS_Y);
    u16* HID = (u16*)(ws + WS_HID);

    prep_kernel<<<1024, 256, 0, stream>>>(x, xn, ws);
    hipFuncSetAttribute((const void*)recur_kernel, hipFuncAttributeMaxDynamicSharedMemorySize, L_TOTAL);
    recur_kernel<<<256, 512, L_TOTAL, stream>>>(mem0, Wq, Wk, Wv, Wo, xn, Qx, Kx, Vx, ws);
    float* PART = (float*)(ws + WS_PART);
    red_ln_kernel<4><<<S_LEN / 4, 256, 0, stream>>>(PART, x, H, Y);
    gemm_kernel<128, 128, 2><<<dim3(FF / 128, S_LEN / 128), 256, 0, stream>>>(Y, DM, w1, w1, w1, FF, HID, HID, HID, FF, DM, 1, b1, nullptr, nullptr);
    gemm_kernel<64, 64, 3><<<dim3(DM / 64, S_LEN / 64), 256, 0, stream>>>(HID, FF, w2, w2, w2, DM, out, out, out, DM, FF, 1, b2, H, nullptr);
}
```

```cpp
#include <hip/hip_runtime.h>
#include <hip/hip_bf16.h>
#include <stdint.h>

#define S_LEN 2048
#define DM 640
#define NH 32
#define DH 192
#define HD 6144
#define MM 8
#define SEG 32
#define NSEG 64
#define FF 1536

typedef unsigned short u16;
typedef __attribute__((ext_vector_type(8))) short bf8;
typedef __attribute__((ext_vector_type(4))) float f4;
typedef __attribute__((ext_vector_type(4))) unsigned u4;
typedef __attribute__((ext_vector_type(2))) unsigned u2;
typedef __attribute__((ext_vector_type(4))) unsigned short us4;

#define SENT 0xFFFFFFFFu

__device__ __forceinline__ float bf2f(u16 v) { return __builtin_bit_cast(float, (unsigned)v << 16); }
typedef __attribute__((ext_vector_type(2))) __bf16 bfv2;
__device__ __forceinline__ unsigned pack2(float a, float b) {
    bfv2 t = {(__bf16)a, (__bf16)b};
    return __builtin_bit_cast(unsigned, t);
}
__device__ __forceinline__ float rbf(float f) { return __builtin_bit_cast(float, pack2(0.0f, f) & 0xFFFF0000u); }
__device__ __forceinline__ unsigned nosent(unsigned w) { return w == SENT ? 0x7FC07FC0u : w; }

__device__ __forceinline__ f4 mfma16(bf8 a, bf8 b, f4 c) { return __builtin_amdgcn_mfma_f32_16x16x32_bf16(a, b, c, 0, 0, 0); }

__device__ __forceinline__ u2 tr_read(unsigned lds_addr) {
    u2 r;
    asm volatile("ds_read_b64_tr_b16 %0, %1\n\ts_waitcnt lgkmcnt(0)" : "=&v"(r) : "v"(lds_addr) : "memory");
    return r;
}
__device__ __forceinline__ bf8 tr_read2(unsigned a0, unsigned a1) {
    u2 r0, r1;
    asm volatile("ds_read_b64_tr_b16 %0, %2\n\tds_read_b64_tr_b16 %1, %3\n\ts_waitcnt lgkmcnt(0)"
                 : "=&v"(r0), "=&v"(r1) : "v"(a0), "v"(a1) : "memory");
    u4 t = {r0.x, r0.y, r1.x, r1.y};
    return __builtin_bit_cast(bf8, t);
}

template <int NT, int HALF>
__device__ __forceinline__ void tr_read_frags(unsigned a0, bf8 (&f)[NT]) {
    static_assert(NT >= 2 && NT <= 4, "NT");
    u2 r[8];
    if constexpr (NT == 2) {
        asm volatile("ds_read_b64_tr_b16 %0, %4 offset:%5\n\tds_read_b64_tr_b16 %1, %4 offset:%6\n\t"
                     "ds_read_b64_tr_b16 %2, %4 offset:%7\n\tds_read_b64_tr_b16 %3, %4 offset:%8\n\ts_waitcnt lgkmcnt(0)"
                     : "=&v"(r[0]), "=&v"(r[1]), "=&v"(r[2]), "=&v"(r[3])
                     : "v"(a0), "i"(0), "i"(HALF), "i"(32), "i"(32 + HALF) : "memory");
    } else if constexpr (NT == 3) {
        asm volatile("ds_read_b64_tr_b16 %0, %6 offset:%7\n\tds_read_b64_tr_b16 %1, %6 offset:%8\n\t"
                     "ds_read_b64_tr_b16 %2, %6 offset:%9\n\tds_read_b64_tr_b16 %3, %6 offset:%10\n\t"
                     "ds_read_b64_tr_b16 %4, %6 offset:%11\n\tds_read_b64_tr_b16 %5, %6 offset:%12\n\ts_waitcnt lgkmcnt(0)"
                     : "=&v"(r[0]), "=&v"(r[1]), "=&v"(r[2]), "=&v"(r[3]), "=&v"(r[4]), "=&v"(r[5])
                     : "v"(a0), "i"(0), "i"(HALF), "i"(32), "i"(32 + HALF), "i"(64), "i"(64 + HALF) : "memory");
    } else {
        asm volatile("ds_read_b64_tr_b16 %0, %8 offset:%9\n\tds_read_b64_tr_b16 %1, %8 offset:%10\n\t"
                     "ds_read_b64_tr_b16 %2, %8 offset:%11\n\tds_read_b64_tr_b16 %3, %8 offset:%12\n\t"
                     "ds_read_b64_tr_b16 %4, %8 offset:%13\n\tds_read_b64_tr_b16 %5, %8 offset:%14\n\t"
                     "ds_read_b64_tr_b16 %6, %8 offset:%15\n\tds_read_b64_tr_b16 %7, %8 offset:%16\n\ts_waitcnt lgkmcnt(0)"
                     : "=&v"(r[0]), "=&v"(r[1]), "=&v"(r[2]), "=&v"(r[3]), "=&v"(r[4]), "=&v"(r[5]), "=&v"(r[6]), "=&v"(r[7])
                     : "v"(a0), "i"(0), "i"(HALF), "i"(32), "i"(32 + HALF), "i"(64), "i"(64 + HALF), "i"(96), "i"(96 + HALF) : "memory");
    }
#pragma unroll
    for (int i = 0; i < NT; ++i) { u4 t = {r[2 * i].x, r[2 * i].y, r[2 * i + 1].x, r[2 * i + 1].y}; f[i] = __builtin_bit_cast(bf8, t); }
}

__device__ __forceinline__ __amdgpu_buffer_rsrc_t mkrsrc(const void* p, unsigned bytes) {
    return __builtin_amdgcn_make_buffer_rsrc((void*)p, 0, bytes, 0x00020000);
}
__device__ __forceinline__ u4 ld16_sc1(__amdgpu_buffer_rsrc_t r, int off) {
    return __builtin_bit_cast(u4, __builtin_amdgcn_raw_buffer_load_b128(r, off, 0, 16));
}
__device__ __forceinline__ void st16_sc1(__amdgpu_buffer_rsrc_t r, int off, u4 v) {
    __builtin_amdgcn_raw_buffer_store_b128(v, r, off, 0, 16);
}
__device__ __forceinline__ void st8_sc1(__amdgpu_buffer_rsrc_t r, int off, u2 v) {
    __builtin_amdgcn_raw_buffer_store_b64(v, r, off, 0, 16);
}
__device__ __forceinline__ bool ok4(u4 v) { return v.x != SENT && v.y != SENT && v.z != SENT && v.w != SENT; }

#define WS_XN    (0ull)
#define WS_Q     (WS_XN + 2048ull * 640 * 2)
#define WS_K     (WS_Q + 2048ull * 6144 * 2)
#define WS_V     (WS_K + 2048ull * 6144 * 2)
#define WS_O     (WS_V + 2048ull * 6144 * 2)
#define WS_H     (WS_O + 2048ull * 6144 * 2)
#define WS_Y     (WS_H + 2048ull * 640 * 2)
#define WS_HID   (WS_Y + 2048ull * 640 * 2)
#define WS_PART  (WS_HID + 2048ull * 1536 * 2)
#define WS_SENT0 (WS_PART + 4ull * 2048 * 640 * 4)
#define E1_REP   8
#define E1_ONE   (5120ull * 2)
#define E1_STEP  (E1_ONE * E1_REP)
#define E2_STEP  (32ull * 3 * 8 * 192 * 2)
#define E3_STEP  (4ull * 32 * 32 * 40 * 4)
#define WS_E1    (WS_SENT0)
#define WS_E2    (WS_E1 + 65 * E1_STEP)
#define WS_E3    (WS_E2 + 64 * E2_STEP)
#define E3_RING  4
#define WS_SENT1 (WS_E3 + E3_RING * E3_STEP)
#define WS_TMO   (WS_SENT1)
#define WS_FLAG  (WS_TMO + 1024)
#define WS_CNTA  (WS_FLAG + 16 * 32 * 64)
#define WS_ZEND  (WS_CNTA + 16 * 64)

__device__ __forceinline__ float wave_sum(float v) {
#pragma unroll
    for (int o = 32; o >= 1; o >>= 1) v += __shfl_xor(v, o);
    return v;
}
__device__ __forceinline__ void ln_row(float (&x)[10]) {
    float s = 0.f;
#pragma unroll
    for (int i = 0; i < 10; ++i) s += x[i];
    s = wave_sum(s);
    const float mean = s * (1.0f / DM);
    float q = 0.f;
#pragma unroll
    for (int i = 0; i < 10; ++i) { float d = x[i] - mean; q += d * d; }
    q = wave_sum(q);
    const float mb = rbf(mean);
    const float vb = rbf(q * (1.0f / DM));
    const float den = rbf(sqrtf(rbf(vb + 1e-5f)));
#pragma unroll
    for (int i = 0; i < 10; ++i) x[i] = rbf(rbf(x[i] - mb) / den);
}

__global__ void __launch_bounds__(256) prep_kernel(const u16* __restrict__ x, u16* __restrict__ y, char* __restrict__ ws) {
    if (blockIdx.x < S_LEN / 4) {
        const int row = blockIdx.x * 4 + (threadIdx.x >> 6), lane = threadIdx.x & 63;
        const unsigned* xr = (const unsigned*)(x + (size_t)row * DM);
        float v[10];
#pragma unroll
        for (int i = 0; i < 5; ++i) { unsigned w = xr[i * 64 + lane]; v[2 * i] = bf2f((u16)(w & 0xFFFF)); v[2 * i + 1] = bf2f((u16)(w >> 16)); }
        ln_row(v);
        unsigned* yr = (unsigned*)(y + (size_t)row * DM);
#pragma unroll
        for (int i = 0; i < 5; ++i) yr[i * 64 + lane] = pack2(v[2 * i], v[2 * i + 1]);
    }
    const size_t gt = (size_t)blockIdx.x * 256 + threadIdx.x, nt = (size_t)gridDim.x * 256;
    u4* ps = (u4*)(ws + WS_SENT0);
    const size_t ns = (WS_SENT1 - WS_SENT0) / 16;
    for (size_t i = gt; i < ns; i += nt) ps[i] = (u4){SENT, SENT, SENT, SENT};
    u4* pz = (u4*)(ws + WS_TMO);
    const size_t nz = (WS_ZEND - WS_TMO) / 16;
    for (size_t i = gt; i < nz; i += nt) pz[i] = (u4){0u, 0u, 0u, 0u};
}

template <int NSPLIT>
__global__ void __launch_bounds__(256) red_ln_kernel(const float* __restrict__ part, const u16* __restrict__ x, u16* __restrict__ hout, u16* __restrict__ y) {
    const int row = blockIdx.x * 4 + (threadIdx.x >> 6), lane = threadIdx.x & 63;
    const unsigned* xr = (const unsigned*)(x + (size_t)row * DM);
    float v[10];
#pragma unroll
    for (int i = 0; i < 5; ++i) {
        float2 a = *(const float2*)(part + (size_t)row * DM + (i * 64 + lane) * 2);
#pragma unroll
        for (int sp = 1; sp < NSPLIT; ++sp) { float2 t = *(const float2*)(part + ((size_t)sp * S_LEN + row) * DM + (i * 64 + lane) * 2); a.x += t.x; a.y += t.y; }
        unsigned w = xr[i * 64 + lane];
        v[2 * i] = rbf(bf2f((u16)(w & 0xFFFF)) + rbf(a.x));
        v[2 * i + 1] = rbf(bf2f((u16)(w >> 16)) + rbf(a.y));
    }
    unsigned* hr = (unsigned*)(hout + (size_t)row * DM);
#pragma unroll
    for (int i = 0; i < 5; ++i) hr[i * 64 + lane] = pack2(v[2 * i], v[2 * i + 1]);
    ln_row(v);
    ln_row(v);
    unsigned* yr = (unsigned*)(y + (size_t)row * DM);
#pragma unroll
    for (int i = 0; i < 5; ++i) yr[i * 64 + lane] = pack2(v[2 * i], v[2 * i + 1]);
}

__device__ __forceinline__ float gelu_tanh(float x) {
    const float k0 = 0.7978845608028654f, k1 = 0.044715f;
    float inner = k0 * (x + k1 * x * x * x);
    return 0.5f * x * (1.0f + tanhf(inner));
}

template <int BM, int BN, int EPI>
__global__ void __launch_bounds__(256) gemm_kernel(const u16* __restrict__ A, int lda,
                                                   const u16* __restrict__ B0, const u16* __restrict__ B1, const u16* __restrict__ B2, int ldb,
                                                   u16* __restrict__ C0, u16* __restrict__ C1, u16* __restrict__ C2, int ldc,
                                                   int K, int nblk_per_mat,
                                                   const u16* __restrict__ aux0, const u16* __restrict__ aux1, float* __restrict__ part) {
    constexpr int BK = 64;
    constexpr int XS = BK * 2 + 16;
    constexpr int WSR = BN * 2 + 32;
    constexpr int XT = BM * XS, WT = BK * WSR;
    constexpr int WM = BM / 2, WN = BN / 2;
    constexpr int TM = WM / 16, TN = WN / 16;
    constexpr int XCH = BM * BK / 8 / 256;
    constexpr int WCH = BK * BN / 8 / 256;
    __shared__ __attribute__((aligned(16))) char smem[2 * (XT + WT)];

    const int tid = threadIdx.x, lane = tid & 63, wave = tid >> 6;
    const int wm = wave >> 1, wn = wave & 1;
    int nb = blockIdx.x, mat = 0;
    if (EPI == 0) { mat = nb / nblk_per_mat; nb -= mat * nblk_per_mat; }
    const u16* B = mat == 0 ? B0 : (mat == 1 ? B1 : B2);
    u16* C = mat == 0 ? C0 : (mat == 1 ? C1 : C2);
    const int m0 = blockIdx.y * BM, n0 = nb * BN;
    if (EPI == 4) { A += (size_t)blockIdx.z * K; B += (size_t)blockIdx.z * K * ldb; }

    u4 xr[XCH], wr[WCH];
    auto gload = [&](int k0) {
#pragma unroll
        for (int i = 0; i < XCH; ++i) {
            int c = tid + i * 256, r = c >> 3, kc = c & 7;
            xr[i] = *(const u4*)(A + (size_t)(m0 + r) * lda + k0 + kc * 8);
        }
#pragma unroll
        for (int i = 0; i < WCH; ++i) {
            int c = tid + i * 256, r = c / (BN / 8), nc = c % (BN / 8);
            wr[i] = *(const u4*)(B + (size_t)(k0 + r) * ldb + n0 + nc * 8);
        }
    };
    auto swrite = [&](int buf) {
        char* xs = smem + buf * (XT + WT); char* ws = xs + XT;
#pragma unroll
        for (int i = 0; i < XCH; ++i) {
            int c = tid + i * 256, r = c >> 3, kc = c & 7;
            *(u4*)(xs + r * XS + kc * 16) = xr[i];
        }
#pragma unroll
        for (int i = 0; i < WCH; ++i) {
            int c = tid + i * 256, r = c / (BN / 8), nc = c % (BN / 8);
            *(u4*)(ws + r * WSR + nc * 16) = wr[i];
        }
    };

    f4 acc[TN][TM];
#pragma unroll
    for (int i = 0; i < TN; ++i)
#pragma unroll
        for (int j = 0; j < TM; ++j) acc[i][j] = (f4){0.f, 0.f, 0.f, 0.f};

    const int g = lane >> 4, li = lane & 15, q = li >> 2, p = li & 3;
    const int nk = K / BK;
    gload(0);
    swrite(0);
    __syncthreads();
    for (int kt = 0; kt < nk; ++kt) {
        const int buf = kt & 1;
        if (kt + 1 < nk) gload((kt + 1) * BK);
        const char* xs = smem + buf * (XT + WT);
        const unsigned wsb = (unsigned)(size_t)(smem + buf * (XT + WT) + XT);
#pragma unroll
        for (int ks = 0; ks < 2; ++ks) {
            bf8 wf[TN], xf[TM];
#pragma unroll
            for (int j = 0; j < TM; ++j)
                xf[j] = *(const bf8*)(xs + (wm * WM + j * 16 + li) * XS + (ks * 32 + 8 * g) * 2);
            tr_read_frags<TN, 4 * WSR>(wsb + (ks * 32 + 8 * g + q) * WSR + (wn * WN + 4 * p) * 2, wf);
#pragma unroll
            for (int i = 0; i < TN; ++i)
#pragma unroll
                for (int j = 0; j < TM; ++j) acc[i][j] = mfma16(wf[i], xf[j], acc[i][j]);
        }
        if (kt + 1 < nk) swrite(buf ^ 1);
        __syncthreads();
    }
#pragma unroll
    for (int i = 0; i < TN; ++i)
#pragma unroll
        for (int j = 0; j < TM; ++j) {
            const int m = m0 + wm * WM + j * 16 + li;
            const int n = n0 + wn * WN + i * 16 + 4 * g;
            if (EPI == 4) { *(f4*)(part + ((size_t)blockIdx.z * S_LEN + m) * ldc + n) = acc[i][j]; continue; }
            float v[4] = {acc[i][j].x, acc[i][j].y, acc[i][j].z, acc[i][j].w};
            if (EPI == 1) {
                us4 xv = *(const us4*)(aux0 + (size_t)m * ldc + n);
#pragma unroll
                for (int e = 0; e < 4; ++e) v[e] = bf2f(xv[e]) + rbf(v[e]);
            } else if (EPI == 2) {
                us4 bv = *(const us4*)(aux0 + n);
#pragma unroll
                for (int e = 0; e < 4; ++e) v[e] = gelu_tanh(rbf(rbf(v[e]) + bf2f(bv[e])));
            } else if (EPI == 3) {
                us4 bv = *(const us4*)(aux0 + n);
                us4 hv = *(const us4*)(aux1 + (size_t)m * ldc + n);
#pragma unroll
                for (int e = 0; e < 4; ++e) v[e] = bf2f(hv[e]) + rbf(rbf(v[e]) + bf2f(bv[e]));
            }
            u2 o = {pack2(v[0], v[1]), pack2(v[2], v[3])};
            *(u2*)(C + (size_t)m * ldc + n) = o;
        }
}

#define KV_BUF  (32 * 384)
#define L_KX    0
#define L_VX    (L_KX + 2 * KV_BUF)
#define L_RED   (L_VX + 2 * KV_BUF)
#define L_RED2  (L_RED + 18432)
#define L_QK    (L_RED2 + 20480)
#define L_STAGE (L_QK + 9600)
#define L_TOTAL (L_STAGE + 51200)
#define SPIN_MAX (1u << 21)
#define DLY1 18
#define DLY2 14
#define DLY3 16
#define DELAY(n) do { for (int d_ = 0; d_ < (n); ++d_) __builtin_amdgcn_s_sleep(1); } while (0)
#define ADUP 3
#define ADAPT(d, spins) do {} while (0)
#define STW(i) do {} while (0)
#define STDUMP() do {} while (0)
#define BAR() asm volatile("s_waitcnt lgkmcnt(0)\n\ts_barrier" ::: "memory")
typedef __attribute__((ext_vector_type(4))) short bf4;

__device__ __forceinline__ f4 mfma16k16(u2 a, u2 b, f4 c) {
    u4 a4 = {a.x, a.y, 0u, 0u}, b4 = {b.x, b.y, 0u, 0u};
    return mfma16(__builtin_bit_cast(bf8, a4), __builtin_bit_cast(bf8, b4), c);
}
__device__ __forceinline__ u2 ld8_sc1(__amdgpu_buffer_rsrc_t r, int off) {
    return __builtin_bit_cast(u2, __builtin_amdgcn_raw_buffer_load_b64(r, off, 0, 16));
}
__device__ __forceinline__ float xmax16(float v) {
    unsigned u = __builtin_bit_cast(unsigned, v);
    auto r = __builtin_amdgcn_permlane16_swap(u, u, false, false);
    return fmaxf(__builtin_bit_cast(float, (unsigned)r[0]), __builtin_bit_cast(float, (unsigned)r[1]));
}
__device__ __forceinline__ float xmax32(float v) {
    unsigned u = __builtin_bit_cast(unsigned, v);
    auto r = __builtin_amdgcn_permlane32_swap(u, u, false, false);
    return fmaxf(__builtin_bit_cast(float, (unsigned)r[0]), __builtin_bit_cast(float, (unsigned)r[1]));
}
__device__ __forceinline__ float xsum16(float v) {
    unsigned u = __builtin_bit_cast(unsigned, v);
    auto r = __builtin_amdgcn_permlane16_swap(u, u, false, false);
    return __builtin_bit_cast(float, (unsigned)r[0]) + __builtin_bit_cast(float, (unsigned)r[1]);
}
__device__ __forceinline__ float xsum32(float v) {
    unsigned u = __builtin_bit_cast(unsigned, v);
    auto r = __builtin_amdgcn_permlane32_swap(u, u, false, false);
    return __builtin_bit_cast(float, (unsigned)r[0]) + __builtin_bit_cast(float, (unsigned)r[1]);
}
template <int CTRL>
__device__ __forceinline__ float dpp_add(float v) {
    int t = __builtin_amdgcn_update_dpp(0, __builtin_bit_cast(int, v), CTRL, 0xf, 0xf, true);
    return v + __builtin_bit_cast(float, t);
}
__device__ __forceinline__ float sum32(float v) {
    v = dpp_add<0xB1>(v);
    v = dpp_add<0x4E>(v);
    v = dpp_add<0x141>(v);
    v = dpp_add<0x140>(v);
    return xsum16(v);
}

__device__ __forceinline__ void qkv_gemm_role(char* smem, int pc, const u16* __restrict__ xn,
                                              const u16* __restrict__ Wq, const u16* __restrict__ Wk, const u16* __restrict__ Wv,
                                              const u16* __restrict__ Wo, u16* Qx, u16* Kx, u16* Vx, u16* Og, char* ws) {
    constexpr int BM = 128, BN = 192, BK = 64;
    constexpr int XS = BK * 2 + 16, WSR = BN * 2 + 32;
    constexpr int XT = BM * XS, WT = BK * WSR;
    const int tid = threadIdx.x, lane = tid & 63, wave = __builtin_amdgcn_readfirstlane(tid >> 6);
    const int wm = wave >> 2, wn = wave & 3;
    const int g = lane >> 4, li = lane & 15, q = li >> 2, p = li & 3;
    for (int item = pc; item < 16 * 96; item += 128) {
        const int rb = item / 96, r96 = item % 96, mi = r96 >> 5, hh = r96 & 31;
        const u16* B = mi == 0 ? Wk : (mi == 1 ? Wv : Wq);
        u16* C = mi == 0 ? Kx : (mi == 1 ? Vx : Qx);
        const int m0 = rb * BM, n0 = hh * DH;
        u4 xr[2], wr[3];
        auto gload = [&](int k0) {
#pragma unroll
            for (int i = 0; i < 2; ++i) { int c = tid + i * 512, r = c >> 3, kc = c & 7; xr[i] = *(const u4*)(xn + (size_t)(m0 + r) * DM + k0 + kc * 8); }
#pragma unroll
            for (int i = 0; i < 3; ++i) { int c = tid + i * 512, r = c / 24, nc = c % 24; wr[i] = *(const u4*)(B + (size_t)(k0 + r) * HD + n0 + nc * 8); }
        };
        auto swrite = [&](int buf) {
            char* xs = smem + buf * (XT + WT); char* wsm = xs + XT;
#pragma unroll
            for (int i = 0; i < 2; ++i) { int c = tid + i * 512, r = c >> 3, kc = c & 7; *(u4*)(xs + r * XS + kc * 16) = xr[i]; }
#pragma unroll
            for (int i = 0; i < 3; ++i) { int c = tid + i * 512, r = c / 24, nc = c % 24; *(u4*)(wsm + r * WSR + nc * 16) = wr[i]; }
        };
        f4 acc[3][4];
#pragma unroll
        for (int i = 0; i < 3; ++i)
#pragma unroll
            for (int jj = 0; jj < 4; ++jj) acc[i][jj] = (f4){0.f, 0.f, 0.f, 0.f};
        gload(0);
        swrite(0);
        __syncthreads();
        for (int kt = 0; kt < DM / BK; ++kt) {
            const int buf = kt & 1;
            if (kt + 1 < DM / BK) gload((kt + 1) * BK);
            const char* xs = smem + buf * (XT + WT);
            const unsigned wsb = (unsigned)(size_t)(smem + buf * (XT + WT) + XT);
#pragma unroll
            for (int ks = 0; ks < 2; ++ks) {
                bf8 wf[3], xf[4];
#pragma unroll
                for (int jj = 0; jj < 4; ++jj) xf[jj] = *(const bf8*)(xs + (wm * 64 + jj * 16 + li) * XS + (ks * 32 + 8 * g) * 2);
                tr_read_frags<3, 4 * WSR>(wsb + (ks * 32 + 8 * g + q) * WSR + (wn * 48 + 4 * p) * 2, wf);
#pragma unroll
                for (int i = 0; i < 3; ++i)
#pragma unroll
                    for (int jj = 0; jj < 4; ++jj) acc[i][jj] = mfma16(wf[i], xf[jj], acc[i][jj]);
            }
            if (kt + 1 < DM / BK) swrite(buf ^ 1);
            __syncthreads();
        }
        __amdgpu_buffer_rsrc_t rc = mkrsrc(C + (size_t)m0 * HD, BM * HD * 2);
#pragma unroll
        for (int i = 0; i < 3; ++i)
#pragma unroll
            for (int jj = 0; jj < 4; ++jj) {
                const int m = wm * 64 + jj * 16 + li, n = n0 + wn * 48 + i * 16 + 4 * g;
                u2 o = {pack2(acc[i][jj].x, acc[i][jj].y), pack2(acc[i][jj].z, acc[i][jj].w)};
                st8_sc1(rc, (m * HD + n) * 2, o);
            }
        asm volatile("s_waitcnt vmcnt(0)" ::: "memory");
        __syncthreads();
        if (tid == 0) __hip_atomic_fetch_add((unsigned*)(ws + WS_FLAG + (rb * 32 + hh) * 64 + (mi == 2 ? 32 : 0)), 1u, __ATOMIC_RELAXED, __HIP_MEMORY_SCOPE_AGENT);
    }
    int* tmo = (int*)(ws + WS_TMO);
    const float scale = 0.07216878364870322f;
    for (int item = pc; item < 16 * 52; item += 128) {
        const int rb = item / 52, it = item % 52;
        __syncthreads();
        if (it >= 32) {
            const int nt = (it - 32) % 5, ks = (it - 32) / 5;
            if (tid == 0) {
                const unsigned* fl = (const unsigned*)(ws + WS_CNTA + rb * 64);
                unsigned spins = 0;
                while (__hip_atomic_load(fl, __ATOMIC_RELAXED, __HIP_MEMORY_SCOPE_AGENT) < 32u) {
                    if (++spins > SPIN_MAX) { *tmo = 8; break; }
                    __builtin_amdgcn_s_sleep(16);
                }
                __builtin_amdgcn_fence(__ATOMIC_ACQUIRE, "agent");
                asm volatile("s_waitcnt vmcnt(0)" ::: "memory");
            }
            __syncthreads();
            constexpr int OWSR = 128 * 2 + 32, OWT = BK * OWSR;
            __amdgpu_buffer_rsrc_t ro = mkrsrc(Og + (size_t)rb * 128 * HD, 128 * HD * 2);
            const u16* Bw = Wo + (size_t)ks * 1536 * DM + nt * 128;
            u4 xr[2][2], wr[2][2];
            auto gload = [&](int k0, u4 (&xq)[2], u4 (&wq)[2]) {
#pragma unroll
                for (int i = 0; i < 2; ++i) { int c = tid + i * 512, r = c >> 3, kc = c & 7; xq[i] = *(const u4*)(Og + (size_t)(rb * 128 + r) * HD + ks * 1536 + k0 + kc * 8); }
#pragma unroll
                for (int i = 0; i < 2; ++i) { int c = tid + i * 512, r = c >> 4, nc = c & 15; wq[i] = *(const u4*)(Bw + (size_t)(k0 + r) * DM + nc * 8); }
            };
            auto swrite = [&](int buf, u4 (&xq)[2], u4 (&wq)[2]) {
                char* xs = smem + buf * (XT + OWT); char* wsm = xs + XT;
#pragma unroll
                for (int i = 0; i < 2; ++i) { int c = tid + i * 512, r = c >> 3, kc = c & 7; *(u4*)(xs + r * XS + kc * 16) = xq[i]; }
#pragma unroll
                for (int i = 0; i < 2; ++i) { int c = tid + i * 512, r = c >> 4, nc = c & 15; *(u4*)(wsm + r * OWSR + nc * 16) = wq[i]; }
            };
            f4 acc[2][4];
#pragma unroll
            for (int i = 0; i < 2; ++i)
#pragma unroll
                for (int jj = 0; jj < 4; ++jj) acc[i][jj] = (f4){0.f, 0.f, 0.f, 0.f};
            auto compute = [&](int buf) {
                const char* xs = smem + buf * (XT + OWT);
                const unsigned wsb = (unsigned)(size_t)(smem + buf * (XT + OWT) + XT);
#pragma unroll
                for (int k2 = 0; k2 < 2; ++k2) {
                    bf8 wf[2], xf[4];
#pragma unroll
                    for (int jj = 0; jj < 4; ++jj) xf[jj] = *(const bf8*)(xs + (wm * 64 + jj * 16 + li) * XS + (k2 * 32 + 8 * g) * 2);
                    tr_read_frags<2, 4 * OWSR>(wsb + (k2 * 32 + 8 * g + q) * OWSR + (wn * 32 + 4 * p) * 2, wf);
#pragma unroll
                    for (int i = 0; i < 2; ++i)
#pragma unroll
                        for (int jj = 0; jj < 4; ++jj) acc[i][jj] = mfma16(wf[i], xf[jj], acc[i][jj]);
                }
            };
            gload(0, xr[0], wr[0]);
            gload(BK, xr[1], wr[1]);
            swrite(0, xr[0], wr[0]);
            BAR();
            for (int kt = 0; kt < 24; kt += 2) {
                if (kt + 2 < 24) gload((kt + 2) * BK, xr[0], wr[0]);
                compute(0);
                swrite(1, xr[1], wr[1]);
                BAR();
                if (kt + 3 < 24) gload((kt + 3) * BK, xr[1], wr[1]);
                compute(1);
                if (kt + 2 < 24) swrite(0, xr[0], wr[0]);
                BAR();
            }
            float* part = (float*)(ws + WS_PART);
#pragma unroll
            for (int i = 0; i < 2; ++i)
#pragma unroll
                for (int jj = 0; jj < 4; ++jj) {
                    const int m = rb * 128 + wm * 64 + jj * 16 + li, n = nt * 128 + wn * 32 + i * 16 + 4 * g;
                    *(f4*)(part + ((size_t)ks * S_LEN + m) * DM + n) = acc[i][jj];
                }
            continue;
        }
        const int hh = it;
        if (tid == 0) {
            const unsigned* fl = (const unsigned*)(ws + WS_FLAG + (rb * 32 + hh) * 64);
            unsigned spins = 0;
            while (__hip_atomic_load(fl, __ATOMIC_RELAXED, __HIP_MEMORY_SCOPE_AGENT) < 2u ||
                   __hip_atomic_load(fl + 8, __ATOMIC_RELAXED, __HIP_MEMORY_SCOPE_AGENT) < 1u) {
                if (++spins > SPIN_MAX) { *tmo = 5; break; }
                __builtin_amdgcn_s_sleep(8);
            }
            const unsigned* ew = (const unsigned*)(ws + WS_E2 + (size_t)(rb * 4 + 3) * E2_STEP + (size_t)hh * 9216 + 9212);
            spins = 0;
            while (__hip_atomic_load(ew, __ATOMIC_RELAXED, __HIP_MEMORY_SCOPE_AGENT) == SENT) {
                if (++spins > SPIN_MAX) { *tmo = 7; break; }
                __builtin_amdgcn_s_sleep(48);
            }
        }
        __syncthreads();
        __amdgpu_buffer_rsrc_t rq = mkrsrc(Qx + (size_t)rb * 128 * HD, 128 * HD * 2);
        __amdgpu_buffer_rsrc_t rk = mkrsrc(Kx + (size_t)rb * 128 * HD, 128 * HD * 2);
        __amdgpu_buffer_rsrc_t rv = mkrsrc(Vx + (size_t)rb * 128 * HD, 128 * HD * 2);
        const int sl = wave >> 1, s = rb * 4 + sl;
        __amdgpu_buffer_rsrc_t re = mkrsrc(ws + WS_E2 + (size_t)s * E2_STEP + (size_t)hh * 9216, 9216);
        char* sv = smem + sl * (48 * 400);
        const int t2 = tid & 127;
        for (int c = t2; c < 32 * 24; c += 128) {
            int r = c / 24, cc = c % 24;
            *(u4*)(sv + r * 400 + cc * 16) = ld16_sc1(rv, ((sl * 32 + r) * HD + hh * DH + cc * 8) * 2);
        }
        for (int c = t2; c < 8 * 24; c += 128) *(u4*)(sv + (40 + c / 24) * 400 + (c % 24) * 16) = (u4){0u, 0u, 0u, 0u};
        bf8 km[6];
        {
            const int c0 = t2, c1 = t2 + 128 < 192 ? t2 + 128 : t2;
            const int o0 = ((24 + ((c0 % 24) >> 1)) * 128 + (c0 / 24) * 16 + (c0 & 1) * 8) * 2;
            const int o1 = ((24 + ((c1 % 24) >> 1)) * 128 + (c1 / 24) * 16 + (c1 & 1) * 8) * 2;
            const int ko = ((12 + (g >> 1)) * 128 + (li & 7) * 16 + (g & 1) * 8) * 2;
            unsigned spins = 0;
            u4 v0, v1;
            for (;;) {
                asm volatile("" ::: "memory");
                u4 kk4[6]; bool ok = true;
                v0 = ld16_sc1(re, o0); v1 = ld16_sc1(re, o1);
#pragma unroll
                for (int kk = 0; kk < 6; ++kk) kk4[kk] = ld16_sc1(re, ko + kk * 512);
#pragma unroll
                for (int kk = 0; kk < 6; ++kk) { ok &= ok4(kk4[kk]); km[kk] = __builtin_bit_cast(bf8, kk4[kk]); }
                if (__all(ok && ok4(v0) && ok4(v1))) break;
                if (++spins > SPIN_MAX) { *tmo = 6; break; }
                __builtin_amdgcn_s_sleep(32);
            }
            *(u4*)(sv + (32 + c0 / 24) * 400 + (c0 % 24) * 16) = v0;
            *(u4*)(sv + (32 + c1 / 24) * 400 + (c1 % 24) * 16) = v1;
        }
        const int row = (wave & 1) * 16 + li;
        f4 sc[3];
#pragma unroll
        for (int t = 0; t < 3; ++t) sc[t] = (f4){0.f, 0.f, 0.f, 0.f};
#pragma unroll
        for (int kk = 0; kk < 6; ++kk) {
            bf8 qf = __builtin_bit_cast(bf8, ld16_sc1(rq, ((sl * 32 + row) * HD + hh * DH + kk * 32 + 8 * g) * 2));
            bf8 k0 = __builtin_bit_cast(bf8, ld16_sc1(rk, ((sl * 32 + li) * HD + hh * DH + kk * 32 + 8 * g) * 2));
            bf8 k1 = __builtin_bit_cast(bf8, ld16_sc1(rk, ((sl * 32 + 16 + li) * HD + hh * DH + kk * 32 + 8 * g) * 2));
            sc[0] = mfma16(k0, qf, sc[0]);
            sc[1] = mfma16(k1, qf, sc[1]);
            sc[2] = mfma16(km[kk], qf, sc[2]);
        }
        float sv2[12];
#pragma unroll
        for (int t = 0; t < 3; ++t)
#pragma unroll
            for (int e = 0; e < 4; ++e) {
                float v = rbf(rbf(sc[t][e]) * scale);
                const int key = t * 16 + 4 * g + e;
                bool valid = t < 2 ? (key <= row) : (g < 2);
                sv2[t * 4 + e] = valid ? v : -INFINITY;
            }
        float mx = sv2[0];
#pragma unroll
        for (int e = 1; e < 12; ++e) mx = fmaxf(mx, sv2[e]);
        mx = xmax32(xmax16(mx));
        float sum = 0.f;
#pragma unroll
        for (int e = 0; e < 12; ++e) { sv2[e] = __expf(sv2[e] - mx); sum += sv2[e]; }
        sum = xsum32(xsum16(sum));
        const float inv = 1.0f / sum;
        u4 pb0 = {pack2(sv2[0] * inv, sv2[1] * inv), pack2(sv2[2] * inv, sv2[3] * inv), pack2(sv2[4] * inv, sv2[5] * inv), pack2(sv2[6] * inv, sv2[7] * inv)};
        u2 pb1 = {pack2(sv2[8] * inv, sv2[9] * inv), pack2(sv2[10] * inv, sv2[11] * inv)};
        const bf8 pf0 = __builtin_bit_cast(bf8, pb0);
        __syncthreads();
        const unsigned lb = (unsigned)(size_t)sv;
        __amdgpu_buffer_rsrc_t rog = mkrsrc(Og + (size_t)rb * 128 * HD, 128 * HD * 2);
#pragma unroll
        for (int dt = 0; dt < 12; ++dt) {
            unsigned a0 = lb + (4 * g + q) * 400 + (dt * 16 + 4 * p) * 2;
            bf8 vf0 = tr_read2(a0, a0 + 16 * 400);
            u2 vm = tr_read(a0 + 32 * 400);
            u4 vt = {vm.x, vm.y, 0u, 0u}, pt = {pb1.x, pb1.y, 0u, 0u};
            f4 a = mfma16(__builtin_bit_cast(bf8, vt), __builtin_bit_cast(bf8, pt), mfma16(vf0, pf0, (f4){0.f, 0.f, 0.f, 0.f}));
            u2 o = {pack2(a.x, a.y), pack2(a.z, a.w)};
            st8_sc1(rog, ((sl * 32 + row) * HD + hh * DH + dt * 16 + 4 * g) * 2, o);
        }
        asm volatile("s_waitcnt vmcnt(0)" ::: "memory");
        __syncthreads();
        if (tid == 0) __hip_atomic_fetch_add((unsigned*)(ws + WS_CNTA + rb * 64), 1u, __ATOMIC_RELAXED, __HIP_MEMORY_SCOPE_AGENT);
    }
}

__global__ void __launch_bounds__(512, 1) recur_kernel(const u16* __restrict__ mem0,
                                                       const u16* __restrict__ Wq, const u16* __restrict__ Wk, const u16* __restrict__ Wv,
                                                       const u16* __restrict__ Wo,
                                                       const u16* __restrict__ xn, u16* Qx, u16* Kx, u16* Vx,
                                                       char* __restrict__ ws) {
    extern __shared__ __attribute__((aligned(16))) char smem[];
    const int tid = threadIdx.x, lane = tid & 63, wave = __builtin_amdgcn_readfirstlane(tid >> 6);
    if (blockIdx.x >= 128) { qkv_gemm_role(smem, blockIdx.x - 128, xn, Wq, Wk, Wv, Wo, Qx, Kx, Vx, (u16*)(ws + WS_O), ws);
        return; }
    const int b = blockIdx.x;
    const int xcd = b & 7, idx = b >> 3;
    const int h = xcd * 4 + (idx >> 2), j = idx & 3;
    const int g = lane >> 4, li = lane & 15, q = li >> 2, p = li & 3;
    const unsigned lbase = (unsigned)(size_t)smem;
    int* tmo = (int*)(ws + WS_TMO);
    const float scale = 0.07216878364870322f;
    int dly1 = DLY1, dly2 = DLY2, dly3 = DLY3;

    auto kv_dma = [&](int s) {
        const int bufo = (s & 1) * KV_BUF;
        if ((s & 3) == 0) {
            const unsigned* fl = (const unsigned*)(ws + WS_FLAG + ((s >> 2) * 32 + h) * 64);
            unsigned spins = 0;
            while (__hip_atomic_load(fl, __ATOMIC_RELAXED, __HIP_MEMORY_SCOPE_AGENT) < 2u) {
                if (++spins > SPIN_MAX) { *tmo = 4; break; }
                __builtin_amdgcn_s_sleep(4);
            }
        }
#pragma unroll 1
        for (int i = 0; i < 12; ++i) {
            const int L = i * 64 + lane, r = L / 24, cp = L % 24, c = (cp & 24) | ((cp ^ r) & 7);
            const size_t goff = (size_t)(s * SEG + r) * HD + h * DH + c * 8;
            __builtin_amdgcn_global_load_lds((const unsigned*)(Kx + goff), (unsigned*)(smem + L_KX + bufo + i * 1024), 16, 0, 16);
            __builtin_amdgcn_global_load_lds((const unsigned*)(Vx + goff), (unsigned*)(smem + L_VX + bufo + i * 1024), 16, 0, 16);
        }
    };

    auto tail = [&](int s) {
        __amdgpu_buffer_rsrc_t r3 = mkrsrc(ws + WS_E3 + (size_t)(s & (E3_RING - 1)) * E3_STEP + (size_t)j * (32 * 32 * 40 * 4), 32 * 32 * 40 * 4);
        if (tid < 320) {
            const int e = tid * 4;
            f4 a = *(const f4*)(smem + L_RED2 + e * 4);
#pragma unroll
            for (int w2 = 1; w2 < 4; ++w2) a += *(const f4*)(smem + L_RED2 + (w2 * 1280 + e) * 4);
            const int dst = e / 40, pos = e % 40;
            const u4 au = __builtin_bit_cast(u4, a);
            u4 o = {nosent(au.x), nosent(au.y), nosent(au.z), nosent(au.w)};
            st16_sc1(r3, ((dst * 32 + h) * 40 + pos) * 4, o);
        }
        STW(5);
        if (wave >= 3) {
            const int pw = wave - 3, src = lane & 31, part = 2 * pw + (lane >> 5);
            u4 v0;
            unsigned spins = 0;
            DELAY(dly3);
            for (;;) {
                asm volatile("" ::: "memory");
                v0 = ld16_sc1(r3, ((h * 32 + src) * 40 + part * 4) * 4);
                if (__all(ok4(v0))) break;
                if (++spins > SPIN_MAX) { *tmo = 3; break; }
            }
            ADAPT(dly3, spins);
            STW(6);
            f4 a = __builtin_bit_cast(f4, v0);
            a.x = sum32(a.x); a.y = sum32(a.y); a.z = sum32(a.z); a.w = sum32(a.w);
            __amdgpu_buffer_rsrc_t r1 = mkrsrc(ws + WS_E1 + (size_t)(s + 1) * E1_STEP, (unsigned)E1_STEP);
            const int row = h >> 2, col = 160 * j + (h & 3) * 40 + 8 * pw;
            unsigned w0 = nosent(pack2(a.x, a.y)), w1 = nosent(pack2(a.z, a.w));
            auto s0 = __builtin_amdgcn_permlane32_swap(w0, w0, false, false);
            auto s1 = __builtin_amdgcn_permlane32_swap(w1, w1, false, false);
            u4 o = {(unsigned)s0[0], (unsigned)s1[0], (unsigned)s0[1], (unsigned)s1[1]};
            if (lane < E1_REP) st16_sc1(r1, lane * (int)E1_ONE + (((col >> 5) * 4 + ((col >> 3) & 3)) * 8 + row) * 16, o);
            st16_sc1(r3, ((h * 32 + src) * 40 + part * 4) * 4, (u4){SENT, SENT, SENT, SENT});
            STW(7);
        }
    };

    if (wave < 4) {
        const int kq = wave;
        bf8 wqkv[9][5];
        {
            constexpr int SS = 320;
#pragma unroll
            for (int kc = 0; kc < 4; ++kc) {
                BAR();
                BAR();
                if (kq == kc) {
#pragma unroll
                    for (int t = 0; t < 9; ++t)
#pragma unroll
                        for (int kk = 0; kk < 5; ++kk) {
                            unsigned a0 = lbase + L_STAGE + (kk * 32 + 8 * g + q) * SS + (t * 16 + 4 * p) * 2;
                            wqkv[t][kk] = tr_read2(a0, a0 + 4 * SS);
                        }
                }
            }
            BAR();
            BAR();
        }
        if (wave == 0) kv_dma(0);
        for (int s = 0; s < NSEG; ++s) {
            STW(0);
            bf8 mf[5];
            const int moff = ((li & 7) * DM + (5 * kq) * 32 + 8 * g) * 2;
            const int foff = ((5 * kq * 4 + g) * 8 + (li & 7)) * 16;
            if (s == 0) {
#pragma unroll
                for (int kk = 0; kk < 5; ++kk) mf[kk] = *(const bf8*)((const char*)mem0 + moff + kk * 64);
                asm volatile("s_waitcnt vmcnt(0)" ::: "memory");
            } else {
                __amdgpu_buffer_rsrc_t r = mkrsrc(ws + WS_E1 + (size_t)s * E1_STEP + (size_t)xcd * E1_ONE, (unsigned)E1_ONE);
                unsigned spins = 0;
                DELAY(dly1);
                for (;;) {
                    asm volatile("" ::: "memory");
                    u4 v[5]; bool ok = true;
#pragma unroll
                    for (int kk = 0; kk < 5; ++kk) v[kk] = ld16_sc1(r, foff + kk * 512);
#pragma unroll
                    for (int kk = 0; kk < 5; ++kk) { ok &= ok4(v[kk]); mf[kk] = __builtin_bit_cast(bf8, v[kk]); }
                    if (__all(ok)) break;
                    if (++spins > SPIN_MAX) { *tmo = 1; break; }
                }
                ADAPT(dly1, spins);
            }
            STW(1);
#pragma unroll
            for (int tg = 0; tg < 2; ++tg) {
                constexpr int NT0 = 5;
                f4 acc[NT0];
#pragma unroll
                for (int t = 0; t < NT0; ++t) acc[t] = (f4){0.f, 0.f, 0.f, 0.f};
#pragma unroll
                for (int kk = 0; kk < 5; ++kk)
#pragma unroll
                    for (int t = 0; t < NT0; ++t) { const int tt = tg * 5 + t; if (tt < 9) acc[t] = mfma16(wqkv[tt < 9 ? tt : 8][kk], mf[kk], acc[t]); }
                if (li < 8) {
#pragma unroll
                    for (int t = 0; t < NT0; ++t) { const int tt = tg * 5 + t; if (tt < 9) *(f4*)(smem + L_RED + (((kq * 9 + tt) * 8 + li) * 16 + 4 * g) * 4) = acc[t]; }
                }
            }
            STW(2);
            BAR();
            STW(3);
            __amdgpu_buffer_rsrc_t r2 = mkrsrc(ws + WS_E2 + (size_t)s * E2_STEP + (size_t)h * (3 * 8 * 192 * 2), 3 * 8 * 192 * 2);
            if (tid < 144) {
                f4 a0 = *(const f4*)(smem + L_RED + tid * 32), a1 = *(const f4*)(smem + L_RED + tid * 32 + 16);
#pragma unroll
                for (int k2 = 1; k2 < 4; ++k2) {
                    a0 += *(const f4*)(smem + L_RED + k2 * 4608 + tid * 32);
                    a1 += *(const f4*)(smem + L_RED + k2 * 4608 + tid * 32 + 16);
                }
                u4 o = {nosent(pack2(a0.x, a0.y)), nosent(pack2(a0.z, a0.w)), nosent(pack2(a1.x, a1.y)), nosent(pack2(a1.z, a1.w))};
                st16_sc1(r2, j * 2304 + tid * 16, o);
            }
            STW(4);
            if (s == NSEG - 1) break;
            BAR();
            if (wave == 0) kv_dma(s + 1);
            BAR();
            tail(s);
            BAR();
        }
        STDUMP();
    } else {
        const int aw = wave - 4;
        bf8 wo0[10]; u2 wo1[10];
        {
            const int t2 = tid - 256;
            u4 stg[15];
#pragma unroll
            for (int kc = 0; kc <= 5; ++kc) {
                if (kc >= 1) {
                    if (kc <= 4) BAR();
#pragma unroll
                    for (int i = 0; i < 15; ++i) {
                        const int c = t2 + i * 256;
                        if (kc - 1 < 4) { if (c < 160 * 18) *(u4*)(smem + L_STAGE + (c / 18) * 320 + (c % 18) * 16) = stg[i]; }
                        else if (c < 192 * 20) *(u4*)(smem + L_KX + (c / 20) * 352 + (c % 20) * 16) = stg[i];
                    }
                }
                if (kc <= 4) {
#pragma unroll
                    for (int i = 0; i < 15; ++i) {
                        const int c = t2 + i * 256;
                        if (kc < 4) {
                            if (c < 160 * 18) {
                                int r = c / 18, cc = c % 18, gt = 9 * j + (cc >> 1), m = gt / 12, col = (gt % 12) * 16 + (cc & 1) * 8;
                                const u16* W = m == 0 ? Wq : (m == 1 ? Wk : Wv);
                                stg[i] = *(const u4*)(W + (size_t)(160 * kc + r) * HD + h * DH + col);
                            }
                        } else if (c < 192 * 20) {
                            int r = c / 20, cc = c % 20;
                            stg[i] = *(const u4*)(Wo + (size_t)(h * DH + r) * DM + 160 * j + cc * 8);
                        }
                    }
                }
                if (kc >= 1) BAR();
            }
#pragma unroll
            for (int ct = 0; ct < 10; ++ct) {
                unsigned a0 = lbase + L_KX + (48 * aw + 4 * g + q) * 352 + (ct * 16 + 4 * p) * 2;
                wo0[ct] = tr_read2(a0, a0 + 16 * 352);
                wo1[ct] = tr_read(a0 + 32 * 352);
            }
            BAR();
        }
        for (int s = 0; s < NSEG; ++s) {
            BAR();
            if (s == NSEG - 1) break;
            const int bufo = (s & 1) * KV_BUF;
            STW(0);
            bf8 kx[2][6], vx[3];
#pragma unroll
            for (int kk = 0; kk < 6; ++kk) {
                const int kc = kk * 4 + g, kcs = ((kc & 24) | ((kc ^ li) & 7)) * 16;
                kx[0][kk] = *(const bf8*)(smem + L_KX + bufo + li * 384 + kcs);
                kx[1][kk] = *(const bf8*)(smem + L_KX + bufo + (16 + li) * 384 + kcs);
            }
#pragma unroll
            for (int dt = 0; dt < 3; ++dt) {
                const int d0 = (3 * aw + dt) * 16;
                const int vr = 4 * g + q, vc = (d0 >> 3) + (p >> 1), vcs = ((vc & 24) | ((vc ^ vr) & 7)) * 16 + (p & 1) * 8;
                unsigned a0 = lbase + L_VX + bufo + vr * 384 + vcs;
                vx[dt] = tr_read2(a0, a0 + 16 * 384);
            }
            STW(1);
            __amdgpu_buffer_rsrc_t r2 = mkrsrc(ws + WS_E2 + (size_t)s * E2_STEP + (size_t)h * (3 * 8 * 192 * 2), 3 * 8 * 192 * 2);
            {
                const int t = tid - 256;
                int cs[3] = {t, t + 256, t + 512 < 576 ? t + 512 : t};
                u4 v[3];
                unsigned spins = 0;
                DELAY(dly2);
                for (;;) {
                    asm volatile("" ::: "memory");
#pragma unroll
                    for (int i = 0; i < 3; ++i) v[i] = ld16_sc1(r2, cs[i] * 16);
                    if (__all(ok4(v[0]) && ok4(v[1]) && ok4(v[2]))) break;
                    if (++spins > SPIN_MAX) { *tmo = 2; break; }
                }
                ADAPT(dly2, spins);
#pragma unroll
                for (int i = 0; i < 3; ++i) {
                    const int gt = cs[i] >> 4, w = cs[i] & 15;
                    *(u4*)(smem + L_QK + ((gt / 12) * 8 + (w >> 1)) * 400 + ((gt % 12) * 16 + (w & 1) * 8) * 2) = v[i];
                }
            }
            BAR();
            bf8 qf[6], km[6]; u2 vm[3];
#pragma unroll
            for (int kk = 0; kk < 6; ++kk) {
                qf[kk] = *(const bf8*)(smem + L_QK + (li & 7) * 400 + (kk * 32 + 8 * g) * 2);
                km[kk] = *(const bf8*)(smem + L_QK + (8 + (li & 7)) * 400 + (kk * 32 + 8 * g) * 2);
            }
#pragma unroll
            for (int dt = 0; dt < 3; ++dt) vm[dt] = tr_read(lbase + L_QK + (16 + ((4 * g + q) & 7)) * 400 + ((3 * aw + dt) * 16 + 4 * p) * 2);
            STW(2);
            f4 sc[3];
#pragma unroll
            for (int t = 0; t < 3; ++t) sc[t] = (f4){0.f, 0.f, 0.f, 0.f};
#pragma unroll
            for (int kk = 0; kk < 6; ++kk) {
                sc[0] = mfma16(kx[0][kk], qf[kk], sc[0]);
                sc[1] = mfma16(kx[1][kk], qf[kk], sc[1]);
                sc[2] = mfma16(km[kk], qf[kk], sc[2]);
            }
            float sv[12];
#pragma unroll
            for (int t = 0; t < 3; ++t)
#pragma unroll
                for (int e = 0; e < 4; ++e) sv[t * 4 + e] = rbf(rbf(sc[t][e]) * scale);
            if (g >= 2) {
#pragma unroll
                for (int e = 0; e < 4; ++e) sv[8 + e] = -INFINITY;
            }
            float mx = sv[0];
#pragma unroll
            for (int e = 1; e < 12; ++e) mx = fmaxf(mx, sv[e]);
            mx = xmax32(xmax16(mx));
            float sum = 0.f;
#pragma unroll
            for (int e = 0; e < 12; ++e) { sv[e] = __expf(sv[e] - mx); sum += sv[e]; }
            sum = xsum32(xsum16(sum));
            const float inv = 1.0f / sum;
            u4 pb0 = {pack2(sv[0] * inv, sv[1] * inv), pack2(sv[2] * inv, sv[3] * inv), pack2(sv[4] * inv, sv[5] * inv), pack2(sv[6] * inv, sv[7] * inv)};
            u2 pb1 = {pack2(sv[8] * inv, sv[9] * inv), pack2(sv[10] * inv, sv[11] * inv)};
            const bf8 pf0 = __builtin_bit_cast(bf8, pb0);
            f4 oa[3];
#pragma unroll
            for (int dt = 0; dt < 3; ++dt) {
                f4 a = mfma16(vx[dt], pf0, (f4){0.f, 0.f, 0.f, 0.f});
                oa[dt] = mfma16k16(vm[dt], pb1, a);
            }
            u4 ob0 = {pack2(oa[0].x, oa[0].y), pack2(oa[0].z, oa[0].w), pack2(oa[1].x, oa[1].y), pack2(oa[1].z, oa[1].w)};
            u2 ob1 = {pack2(oa[2].x, oa[2].y), pack2(oa[2].z, oa[2].w)};
            const bf8 of0 = __builtin_bit_cast(bf8, ob0);
            f4 pa[10];
#pragma unroll
            for (int ct = 0; ct < 10; ++ct) pa[ct] = mfma16k16(wo1[ct], ob1, mfma16(wo0[ct], of0, (f4){0.f, 0.f, 0.f, 0.f}));
            if (li < 8) {
#pragma unroll
                for (int ct = 0; ct < 10; ++ct) *(f4*)(smem + L_RED2 + ((aw * 8 + li) * 160 + ct * 16 + 4 * g) * 4) = pa[ct];
            }
            STW(3);
            BAR();
            STW(4);
            tail(s);
            BAR();
        }
        STDUMP();
    }
}

extern "C" void kernel_launch(void* const* d_in, const int* in_sizes, int n_in,
                              void* d_out, int out_size, void* d_ws, size_t ws_size,
                              hipStream_t stream) {
    const u16* x    = (const u16*)d_in[0];
    const u16* mem0 = (const u16*)d_in[1];
    const u16* Wq   = (const u16*)d_in[2];
    const u16* Wk   = (const u16*)d_in[3];
    const u16* Wv   = (const u16*)d_in[4];
    const u16* Wo   = (const u16*)d_in[5];
    const u16* w1   = (const u16*)d_in[8];
    const u16* b1   = (const u16*)d_in[9];
    const u16* w2   = (const u16*)d_in[10];
    const u16* b2   = (const u16*)d_in[11];
    u16* out = (u16*)d_out;
    char* ws = (char*)d_ws;
    u16* xn = (u16*)(ws + WS_XN);
    u16* Qx = (u16*)(ws + WS_Q);
    u16* Kx = (u16*)(ws + WS_K);
    u16* Vx = (u16*)(ws + WS_V);
    u16* O  = (u16*)(ws + WS_O);
    u16* H  = (u16*)(ws + WS_H);
    u16* Y  = (u16*)(ws + WS_Y);
    u16* HID = (u16*)(ws + WS_HID);

    prep_kernel<<<1024, 256, 0, stream>>>(x, xn, ws);
    (void)hipFuncSetAttribute((const void*)recur_kernel, hipFuncAttributeMaxDynamicSharedMemorySize, L_TOTAL);
    recur_kernel<<<256, 512, L_TOTAL, stream>>>(mem0, Wq, Wk, Wv, Wo, xn, Qx, Kx, Vx, ws);
    float* PART = (float*)(ws + WS_PART);
    red_ln_kernel<4><<<S_LEN / 4, 256, 0, stream>>>(PART, x, H, Y);
    gemm_kernel<128, 128, 2><<<dim3(FF / 128, S_LEN / 128), 256, 0, stream>>>(Y, DM, w1, w1, w1, FF, HID, HID, HID, FF, DM, 1, b1, nullptr, nullptr);
    gemm_kernel<64, 64, 3><<<dim3(DM / 64, S_LEN / 64), 256, 0, stream>>>(HID, FF, w2, w2, w2, DM, out, out, out, DM, FF, 1, b2, H, nullptr);
}
```

```cpp
#include <hip/hip_runtime.h>
#include <hip/hip_bf16.h>
#include <stdint.h>

#define S_LEN 2048
#define DM 640
#define NH 32
#define DH 192
#define HD 6144
#define MM 8
#define SEG 32
#define NSEG 64
#define FF 1536

typedef unsigned short u16;
typedef __attribute__((ext_vector_type(8))) short bf8;
typedef __attribute__((ext_vector_type(4))) float f4;
typedef __attribute__((ext_vector_type(4))) unsigned u4;
typedef __attribute__((ext_vector_type(2))) unsigned u2;
typedef __attribute__((ext_vector_type(4))) unsigned short us4;

#define SENT 0xFFFFFFFFu

__device__ __forceinline__ float bf2f(u16 v) { return __builtin_bit_cast(float, (unsigned)v << 16); }
typedef __attribute__((ext_vector_type(2))) __bf16 bfv2;
__device__ __forceinline__ unsigned pack2(float a, float b) {
    bfv2 t = {(__bf16)a, (__bf16)b};
    return __builtin_bit_cast(unsigned, t);
}
__device__ __forceinline__ float rbf(float f) { return __builtin_bit_cast(float, pack2(0.0f, f) & 0xFFFF0000u); }
__device__ __forceinline__ unsigned nosent(unsigned w) { return w == SENT ? 0x7FC07FC0u : w; }

__device__ __forceinline__ f4 mfma16(bf8 a, bf8 b, f4 c) { return __builtin_amdgcn_mfma_f32_16x16x32_bf16(a, b, c, 0, 0, 0); }

__device__ __forceinline__ u2 tr_read(unsigned lds_addr) {
    u2 r;
    asm volatile("ds_read_b64_tr_b16 %0, %1\n\ts_waitcnt lgkmcnt(0)" : "=&v"(r) : "v"(lds_addr) : "memory");
    return r;
}
__device__ __forceinline__ bf8 tr_read2(unsigned a0, unsigned a1) {
    u2 r0, r1;
    asm volatile("ds_read_b64_tr_b16 %0, %2\n\tds_read_b64_tr_b16 %1, %3\n\ts_waitcnt lgkmcnt(0)"
                 : "=&v"(r0), "=&v"(r1) : "v"(a0), "v"(a1) : "memory");
    u4 t = {r0.x, r0.y, r1.x, r1.y};
    return __builtin_bit_cast(bf8, t);
}

template <int NT, int HALF>
__device__ __forceinline__ void tr_read_frags(unsigned a0, bf8 (&f)[NT]) {
    static_assert(NT >= 2 && NT <= 4, "NT");
    u2 r[8];
    if constexpr (NT == 2) {
        asm volatile("ds_read_b64_tr_b16 %0, %4 offset:%5\n\tds_read_b64_tr_b16 %1, %4 offset:%6\n\t"
                     "ds_read_b64_tr_b16 %2, %4 offset:%7\n\tds_read_b64_tr_b16 %3, %4 offset:%8\n\ts_waitcnt lgkmcnt(0)"
                     : "=&v"(r[0]), "=&v"(r[1]), "=&v"(r[2]), "=&v"(r[3])
                     : "v"(a0), "i"(0), "i"(HALF), "i"(32), "i"(32 + HALF) : "memory");
    } else if constexpr (NT == 3) {
        asm volatile("ds_read_b64_tr_b16 %0, %6 offset:%7\n\tds_read_b64_tr_b16 %1, %6 offset:%8\n\t"
                     "ds_read_b64_tr_b16 %2, %6 offset:%9\n\tds_read_b64_tr_b16 %3, %6 offset:%10\n\t"
                     "ds_read_b64_tr_b16 %4, %6 offset:%11\n\tds_read_b64_tr_b16 %5, %6 offset:%12\n\ts_waitcnt lgkmcnt(0)"
                     : "=&v"(r[0]), "=&v"(r[1]), "=&v"(r[2]), "=&v"(r[3]), "=&v"(r[4]), "=&v"(r[5])
                     : "v"(a0), "i"(0), "i"(HALF), "i"(32), "i"(32 + HALF), "i"(64), "i"(64 + HALF) : "memory");
    } else {
        asm volatile("ds_read_b64_tr_b16 %0, %8 offset:%9\n\tds_read_b64_tr_b16 %1, %8 offset:%10\n\t"
                     "ds_read_b64_tr_b16 %2, %8 offset:%11\n\tds_read_b64_tr_b16 %3, %8 offset:%12\n\t"
                     "ds_read_b64_tr_b16 %4, %8 offset:%13\n\tds_read_b64_tr_b16 %5, %8 offset:%14\n\t"
                     "ds_read_b64_tr_b16 %6, %8 offset:%15\n\tds_read_b64_tr_b16 %7, %8 offset:%16\n\ts_waitcnt lgkmcnt(0)"
                     : "=&v"(r[0]), "=&v"(r[1]), "=&v"(r[2]), "=&v"(r[3]), "=&v"(r[4]), "=&v"(r[5]), "=&v"(r[6]), "=&v"(r[7])
                     : "v"(a0), "i"(0), "i"(HALF), "i"(32), "i"(32 + HALF), "i"(64), "i"(64 + HALF), "i"(96), "i"(96 + HALF) : "memory");
    }
#pragma unroll
    for (int i = 0; i < NT; ++i) { u4 t = {r[2 * i].x, r[2 * i].y, r[2 * i + 1].x, r[2 * i + 1].y}; f[i] = __builtin_bit_cast(bf8, t); }
}

__device__ __forceinline__ __amdgpu_buffer_rsrc_t mkrsrc(const void* p, unsigned bytes) {
    return __builtin_amdgcn_make_buffer_rsrc((void*)p, 0, bytes, 0x00020000);
}
__device__ __forceinline__ u4 ld16_sc1(__amdgpu_buffer_rsrc_t r, int off) {
    return __builtin_bit_cast(u4, __builtin_amdgcn_raw_buffer_load_b128(r, off, 0, 16));
}
__device__ __forceinline__ void st16_sc1(__amdgpu_buffer_rsrc_t r, int off, u4 v) {
    __builtin_amdgcn_raw_buffer_store_b128(v, r, off, 0, 16);
}
__device__ __forceinline__ void st8_sc1(__amdgpu_buffer_rsrc_t r, int off, u2 v) {
    __builtin_amdgcn_raw_buffer_store_b64(v, r, off, 0, 16);
}
__device__ __forceinline__ bool ok4(u4 v) { return v.x != SENT && v.y != SENT && v.z != SENT && v.w != SENT; }

#define WS_XN    (0ull)
#define WS_Q     (WS_XN + 2048ull * 640 * 2)
#define WS_K     (WS_Q + 2048ull * 6144 * 2)
#define WS_V     (WS_K + 2048ull * 6144 * 2)
#define WS_O     (WS_V + 2048ull * 6144 * 2)
#define WS_H     (WS_O + 2048ull * 6144 * 2)
#define WS_Y     (WS_H + 2048ull * 640 * 2)
#define WS_HID   (WS_Y + 2048ull * 640 * 2)
#define WS_PART  (WS_HID + 2048ull * 1536 * 2)
#define WS_SENT0 (WS_PART + 4ull * 2048 * 640 * 4)
#define E1_REP   8
#define E1_ONE   (5120ull * 2)
#define E1_STEP  (E1_ONE * E1_REP)
#define E2_STEP  (32ull * 3 * 8 * 192 * 2)
#define E3_STEP  (4ull * 32 * 32 * 40 * 4)
#define WS_E1    (WS_SENT0)
#define WS_E2    (WS_E1 + 65 * E1_STEP)
#define WS_E3    (WS_E2 + 64 * E2_STEP)
#define E3_RING  4
#define WS_SENT1 (WS_E3 + E3_RING * E3_STEP)
#define WS_TMO   (WS_SENT1)
#define WS_FLAG  (WS_TMO + 1024)
#define WS_CNTA  (WS_FLAG + 16 * 32 * 64)
#define WS_ZEND  (WS_CNTA + 16 * 64)

__device__ __forceinline__ float wave_sum(float v) {
#pragma unroll
    for (int o = 32; o >= 1; o >>= 1) v += __shfl_xor(v, o);
    return v;
}
__device__ __forceinline__ void ln_row(float (&x)[10]) {
    float s = 0.f;
#pragma unroll
    for (int i = 0; i < 10; ++i) s += x[i];
    s = wave_sum(s);
    const float mean = s * (1.0f / DM);
    float q = 0.f;
#pragma unroll
    for (int i = 0; i < 10; ++i) { float d = x[i] - mean; q += d * d; }
    q = wave_sum(q);
    const float mb = rbf(mean);
    const float vb = rbf(q * (1.0f / DM));
    const float den = rbf(sqrtf(rbf(vb + 1e-5f)));
#pragma unroll
    for (int i = 0; i < 10; ++i) x[i] = rbf(rbf(x[i] - mb) / den);
}

__global__ void __launch_bounds__(256) prep_kernel(const u16* __restrict__ x, u16* __restrict__ y, char* __restrict__ ws) {
    if (blockIdx.x < S_LEN / 4) {
        const int row = blockIdx.x * 4 + (threadIdx.x >> 6), lane = threadIdx.x & 63;
        const unsigned* xr = (const unsigned*)(x + (size_t)row * DM);
        float v[10];
#pragma unroll
        for (int i = 0; i < 5; ++i) { unsigned w = xr[i * 64 + lane]; v[2 * i] = bf2f((u16)(w & 0xFFFF)); v[2 * i + 1] = bf2f((u16)(w >> 16)); }
        ln_row(v);
        unsigned* yr = (unsigned*)(y + (size_t)row * DM);
#pragma unroll
        for (int i = 0; i < 5; ++i) yr[i * 64 + lane] = pack2(v[2 * i], v[2 * i + 1]);
    }
    const size_t gt = (size_t)blockIdx.x * 256 + threadIdx.x, nt = (size_t)gridDim.x * 256;
    u4* ps = (u4*)(ws + WS_SENT0);
    const size_t ns = (WS_SENT1 - WS_SENT0) / 16;
    for (size_t i = gt; i < ns; i += nt) ps[i] = (u4){SENT, SENT, SENT, SENT};
    u4* pz = (u4*)(ws + WS_TMO);
    const size_t nz = (WS_ZEND - WS_TMO) / 16;
    for (size_t i = gt; i < nz; i += nt) pz[i] = (u4){0u, 0u, 0u, 0u};
}

template <int NSPLIT>
__global__ void __launch_bounds__(256) red_ln_kernel(const float* __restrict__ part, const u16* __restrict__ x, u16* __restrict__ hout, u16* __restrict__ y) {
    const int row = blockIdx.x * 4 + (threadIdx.x >> 6), lane = threadIdx.x & 63;
    const unsigned* xr = (const unsigned*)(x + (size_t)row * DM);
    float v[10];
#pragma unroll
    for (int i = 0; i < 5; ++i) {
        float2 a = *(const float2*)(part + (size_t)row * DM + (i * 64 + lane) * 2);
#pragma unroll
        for (int sp = 1; sp < NSPLIT; ++sp) { float2 t = *(const float2*)(part + ((size_t)sp * S_LEN + row) * DM + (i * 64 + lane) * 2); a.x += t.x; a.y += t.y; }
        unsigned w = xr[i * 64 + lane];
        v[2 * i] = rbf(bf2f((u16)(w & 0xFFFF)) + rbf(a.x));
        v[2 * i + 1] = rbf(bf2f((u16)(w >> 16)) + rbf(a.y));
    }
    unsigned* hr = (unsigned*)(hout + (size_t)row * DM);
#pragma unroll
    for (int i = 0; i < 5; ++i) hr[i * 64 + lane] = pack2(v[2 * i], v[2 * i + 1]);
    ln_row(v);
    ln_row(v);
    unsigned* yr = (unsigned*)(y + (size_t)row * DM);
#pragma unroll
    for (int i = 0; i < 5; ++i) yr[i * 64 + lane] = pack2(v[2 * i], v[2 * i + 1]);
}

__device__ __forceinline__ float gelu_tanh(float x) {
    const float k0 = 0.7978845608028654f, k1 = 0.044715f;
    float inner = k0 * (x + k1 * x * x * x);
    return 0.5f * x * (1.0f + tanhf(inner));
}

template <int BM, int BN, int EPI>
__global__ void __launch_bounds__(256) gemm_kernel(const u16* __restrict__ A, int lda,
                                                   const u16* __restrict__ B0, const u16* __restrict__ B1, const u16* __restrict__ B2, int ldb,
                                                   u16* __restrict__ C0, u16* __restrict__ C1, u16* __restrict__ C2, int ldc,
                                                   int K, int nblk_per_mat,
                                                   const u16* __restrict__ aux0, const u16* __restrict__ aux1, float* __restrict__ part) {
    constexpr int BK = 64;
    constexpr int XS = BK * 2 + 16;
    constexpr int WSR = BN * 2 + 32;
    constexpr int XT = BM * XS, WT = BK * WSR;
    constexpr int WM = BM / 2, WN = BN / 2;
    constexpr int TM = WM / 16, TN = WN / 16;
    constexpr int XCH = BM * BK / 8 / 256;
    constexpr int WCH = BK * BN / 8 / 256;
    __shared__ __attribute__((aligned(16))) char smem[2 * (XT + WT)];

    const int tid = threadIdx.x, lane = tid & 63, wave = tid >> 6;
    const int wm = wave >> 1, wn = wave & 1;
    int nb = blockIdx.x, mat = 0;
    if (EPI == 0) { mat = nb / nblk_per_mat; nb -= mat * nblk_per_mat; }
    const u16* B = mat == 0 ? B0 : (mat == 1 ? B1 : B2);
    u16* C = mat == 0 ? C0 : (mat == 1 ? C1 : C2);
    const int m0 = blockIdx.y * BM, n0 = nb * BN;
    if (EPI == 4) { A += (size_t)blockIdx.z * K; B += (size_t)blockIdx.z * K * ldb; }

    u4 xr[XCH], wr[WCH];
    auto gload = [&](int k0) {
#pragma unroll
        for (int i = 0; i < XCH; ++i) {
            int c = tid + i * 256, r = c >> 3, kc = c & 7;
            xr[i] = *(const u4*)(A + (size_t)(m0 + r) * lda + k0 + kc * 8);
        }
#pragma unroll
        for (int i = 0; i < WCH; ++i) {
            int c = tid + i * 256, r = c / (BN / 8), nc = c % (BN / 8);
            wr[i] = *(const u4*)(B + (size_t)(k0 + r) * ldb + n0 + nc * 8);
        }
    };
    auto swrite = [&](int buf) {
        char* xs = smem + buf * (XT + WT); char* ws = xs + XT;
#pragma unroll
        for (int i = 0; i < XCH; ++i) {
            int c = tid + i * 256, r = c >> 3, kc = c & 7;
            *(u4*)(xs + r * XS + kc * 16) = xr[i];
        }
#pragma unroll
        for (int i = 0; i < WCH; ++i) {
            int c = tid + i * 256, r = c / (BN / 8), nc = c % (BN / 8);
            *(u4*)(ws + r * WSR + nc * 16) = wr[i];
        }
    };

    f4 acc[TN][TM];
#pragma unroll
    for (int i = 0; i < TN; ++i)
#pragma unroll
        for (int j = 0; j < TM; ++j) acc[i][j] = (f4){0.f, 0.f, 0.f, 0.f};

    const int g = lane >> 4, li = lane & 15, q = li >> 2, p = li & 3;
    const int nk = K / BK;
    gload(0);
    swrite(0);
    __syncthreads();
    for (int kt = 0; kt < nk; ++kt) {
        const int buf = kt & 1;
        if (kt + 1 < nk) gload((kt + 1) * BK);
        const char* xs = smem + buf * (XT + WT);
        const unsigned wsb = (unsigned)(size_t)(smem + buf * (XT + WT) + XT);
#pragma unroll
        for (int ks = 0; ks < 2; ++ks) {
            bf8 wf[TN], xf[TM];
#pragma unroll
            for (int j = 0; j < TM; ++j)
                xf[j] = *(const bf8*)(xs + (wm * WM + j * 16 + li) * XS + (ks * 32 + 8 * g) * 2);
            tr_read_frags<TN, 4 * WSR>(wsb + (ks * 32 + 8 * g + q) * WSR + (wn * WN + 4 * p) * 2, wf);
#pragma unroll
            for (int i = 0; i < TN; ++i)
#pragma unroll
                for (int j = 0; j < TM; ++j) acc[i][j] = mfma16(wf[i], xf[j], acc[i][j]);
        }
        if (kt + 1 < nk) swrite(buf ^ 1);
        __syncthreads();
    }
#pragma unroll
    for (int i = 0; i < TN; ++i)
#pragma unroll
        for (int j = 0; j < TM; ++j) {
            const int m = m0 + wm * WM + j * 16 + li;
            const int n = n0 + wn * WN + i * 16 + 4 * g;
            if (EPI == 4) { *(f4*)(part + ((size_t)blockIdx.z * S_LEN + m) * ldc + n) = acc[i][j]; continue; }
            float v[4] = {acc[i][j].x, acc[i][j].y, acc[i][j].z, acc[i][j].w};
            if (EPI == 1) {
                us4 xv = *(const us4*)(aux0 + (size_t)m * ldc + n);
#pragma unroll
                for (int e = 0; e < 4; ++e) v[e] = bf2f(xv[e]) + rbf(v[e]);
            } else if (EPI == 2) {
                us4 bv = *(const us4*)(aux0 + n);
#pragma unroll
                for (int e = 0; e < 4; ++e) v[e] = gelu_tanh(rbf(rbf(v[e]) + bf2f(bv[e])));
            } else if (EPI == 3) {
                us4 bv = *(const us4*)(aux0 + n);
                us4 hv = *(const us4*)(aux1 + (size_t)m * ldc + n);
#pragma unroll
                for (int e = 0; e < 4; ++e) v[e] = bf2f(hv[e]) + rbf(rbf(v[e]) + bf2f(bv[e]));
            }
            u2 o = {pack2(v[0], v[1]), pack2(v[2], v[3])};
            *(u2*)(C + (size_t)m * ldc + n) = o;
        }
}

#define KV_BUF  (32 * 384)
#define L_KX    0
#define L_VX    (L_KX + 2 * KV_BUF)
#define L_RED   (L_VX + 2 * KV_BUF)
#define L_RED2  (L_RED + 18432)
#define L_QK    (L_RED2 + 20480)
#define L_STAGE (L_QK + 9600)
#define L_TOTAL (L_STAGE + 51200)
#define SPIN_MAX (1u << 21)
#define DLY1 18
#define DLY2 11
#define DLY3 16
#define DELAY(n) do { for (int d_ = 0; d_ < (n); ++d_) __builtin_amdgcn_s_sleep(1); } while (0)
#define ADUP 3
#define ADAPT(d, spins) do {} while (0)
#define STW(i) do {} while (0)
#define STDUMP() do {} while (0)
#define BAR() asm volatile("s_waitcnt lgkmcnt(0)\n\ts_barrier" ::: "memory")
typedef __attribute__((ext_vector_type(4))) short bf4;

__device__ __forceinline__ f4 mfma16k16(u2 a, u2 b, f4 c) {
    u4 a4 = {a.x, a.y, 0u, 0u}, b4 = {b.x, b.y, 0u, 0u};
    return mfma16(__builtin_bit_cast(bf8, a4), __builtin_bit_cast(bf8, b4), c);
}
__device__ __forceinline__ u2 ld8_sc1(__amdgpu_buffer_rsrc_t r, int off) {
    return __builtin_bit_cast(u2, __builtin_amdgcn_raw_buffer_load_b64(r, off, 0, 16));
}
__device__ __forceinline__ float xmax16(float v) {
    unsigned u = __builtin_bit_cast(unsigned, v);
    auto r = __builtin_amdgcn_permlane16_swap(u, u, false, false);
    return fmaxf(__builtin_bit_cast(float, (unsigned)r[0]), __builtin_bit_cast(float, (unsigned)r[1]));
}
__device__ __forceinline__ float xmax32(float v) {
    unsigned u = __builtin_bit_cast(unsigned, v);
    auto r = __builtin_amdgcn_permlane32_swap(u, u, false, false);
    return fmaxf(__builtin_bit_cast(float, (unsigned)r[0]), __builtin_bit_cast(float, (unsigned)r[1]));
}
__device__ __forceinline__ float xsum16(float v) {
    unsigned u = __builtin_bit_cast(unsigned, v);
    auto r = __builtin_amdgcn_permlane16_swap(u, u, false, false);
    return __builtin_bit_cast(float, (unsigned)r[0]) + __builtin_bit_cast(float, (unsigned)r[1]);
}
__device__ __forceinline__ float xsum32(float v) {
    unsigned u = __builtin_bit_cast(unsigned, v);
    auto r = __builtin_amdgcn_permlane32_swap(u, u, false, false);
    return __builtin_bit_cast(float, (unsigned)r[0]) + __builtin_bit_cast(float, (unsigned)r[1]);
}
template <int CTRL>
__device__ __forceinline__ float dpp_add(float v) {
    int t = __builtin_amdgcn_update_dpp(0, __builtin_bit_cast(int, v), CTRL, 0xf, 0xf, true);
    return v + __builtin_bit_cast(float, t);
}
__device__ __forceinline__ float sum32(float v) {
    v = dpp_add<0xB1>(v);
    v = dpp_add<0x4E>(v);
    v = dpp_add<0x141>(v);
    v = dpp_add<0x140>(v);
    return xsum16(v);
}

__device__ __forceinline__ void qkv_gemm_role(char* smem, int pc, const u16* __restrict__ xn,
                                              const u16* __restrict__ Wq, const u16* __restrict__ Wk, const u16* __restrict__ Wv,
                                              const u16* __restrict__ Wo, u16* Qx, u16* Kx, u16* Vx, u16* Og, char* ws) {
    constexpr int BM = 128, BN = 192, BK = 64;
    constexpr int XS = BK * 2 + 16, WSR = BN * 2 + 32;
    constexpr int XT = BM * XS, WT = BK * WSR;
    const int tid = threadIdx.x, lane = tid & 63, wave = __builtin_amdgcn_readfirstlane(tid >> 6);
    const int wm = wave >> 2, wn = wave & 3;
    const int g = lane >> 4, li = lane & 15, q = li >> 2, p = li & 3;
    for (int item = pc; item < 16 * 96; item += 128) {
        const int rb = item / 96, r96 = item % 96, mi = r96 >> 5, hh = r96 & 31;
        const u16* B = mi == 0 ? Wk : (mi == 1 ? Wv : Wq);
        u16* C = mi == 0 ? Kx : (mi == 1 ? Vx : Qx);
        const int m0 = rb * BM, n0 = hh * DH;
        u4 xr[2], wr[3];
        auto gload = [&](int k0) {
#pragma unroll
            for (int i = 0; i < 2; ++i) { int c = tid + i * 512, r = c >> 3, kc = c & 7; xr[i] = *(const u4*)(xn + (size_t)(m0 + r) * DM + k0 + kc * 8); }
#pragma unroll
            for (int i = 0; i < 3; ++i) { int c = tid + i * 512, r = c / 24, nc = c % 24; wr[i] = *(const u4*)(B + (size_t)(k0 + r) * HD + n0 + nc * 8); }
        };
        auto swrite = [&](int buf) {
            char* xs = smem + buf * (XT + WT); char* wsm = xs + XT;
#pragma unroll
            for (int i = 0; i < 2; ++i) { int c = tid + i * 512, r = c >> 3, kc = c & 7; *(u4*)(xs + r * XS + kc * 16) = xr[i]; }
#pragma unroll
            for (int i = 0; i < 3; ++i) { int c = tid + i * 512, r = c / 24, nc = c % 24; *(u4*)(wsm + r * WSR + nc * 16) = wr[i]; }
        };
        f4 acc[3][4];
#pragma unroll
        for (int i = 0; i < 3; ++i)
#pragma unroll
            for (int jj = 0; jj < 4; ++jj) acc[i][jj] = (f4){0.f, 0.f, 0.f, 0.f};
        gload(0);
        swrite(0);
        __syncthreads();
        for (int kt = 0; kt < DM / BK; ++kt) {
            const int buf = kt & 1;
            if (kt + 1 < DM / BK) gload((kt + 1) * BK);
            const char* xs = smem + buf * (XT + WT);
            const unsigned wsb = (unsigned)(size_t)(smem + buf * (XT + WT) + XT);
#pragma unroll
            for (int ks = 0; ks < 2; ++ks) {
                bf8 wf[3], xf[4];
#pragma unroll
                for (int jj = 0; jj < 4; ++jj) xf[jj] = *(const bf8*)(xs + (wm * 64 + jj * 16 + li) * XS + (ks * 32 + 8 * g) * 2);
                tr_read_frags<3, 4 * WSR>(wsb + (ks * 32 + 8 * g + q) * WSR + (wn * 48 + 4 * p) * 2, wf);
#pragma unroll
                for (int i = 0; i < 3; ++i)
#pragma unroll
                    for (int jj = 0; jj < 4; ++jj) acc[i][jj] = mfma16(wf[i], xf[jj], acc[i][jj]);
            }
            if (kt + 1 < DM / BK) swrite(buf ^ 1);
            __syncthreads();
        }
        __amdgpu_buffer_rsrc_t rc = mkrsrc(C + (size_t)m0 * HD, BM * HD * 2);
#pragma unroll
        for (int i = 0; i < 3; ++i)
#pragma unroll
            for (int jj = 0; jj < 4; ++jj) {
                const int m = wm * 64 + jj * 16 + li, n = n0 + wn * 48 + i * 16 + 4 * g;
                u2 o = {pack2(acc[i][jj].x, acc[i][jj].y), pack2(acc[i][jj].z, acc[i][jj].w)};
                st8_sc1(rc, (m * HD + n) * 2, o);
            }
        asm volatile("s_waitcnt vmcnt(0)" ::: "memory");
        __syncthreads();
        if (tid == 0) __hip_atomic_fetch_add((unsigned*)(ws + WS_FLAG + (rb * 32 + hh) * 64 + (mi == 2 ? 32 : 0)), 1u, __ATOMIC_RELAXED, __HIP_MEMORY_SCOPE_AGENT);
    }
    int* tmo = (int*)(ws + WS_TMO);
    const float scale = 0.07216878364870322f;
    for (int item = pc; item < 16 * 52; item += 128) {
        const int rb = item / 52, it = item % 52;
        __syncthreads();
        if (it >= 32) {
            const int nt = (it - 32) % 5, ks = (it - 32) / 5;
            if (tid == 0) {
                const unsigned* fl = (const unsigned*)(ws + WS_CNTA + rb * 64);
                unsigned spins = 0;
                while (__hip_atomic_load(fl, __ATOMIC_RELAXED, __HIP_MEMORY_SCOPE_AGENT) < 32u) {
                    if (++spins > SPIN_MAX) { *tmo = 8; break; }
                    __builtin_amdgcn_s_sleep(16);
                }
                __builtin_amdgcn_fence(__ATOMIC_ACQUIRE, "agent");
                asm volatile("s_waitcnt vmcnt(0)" ::: "memory");
            }
            __syncthreads();
            constexpr int OWSR = 128 * 2 + 32, OWT = BK * OWSR;
            __amdgpu_buffer_rsrc_t ro = mkrsrc(Og + (size_t)rb * 128 * HD, 128 * HD * 2);
            const u16* Bw = Wo + (size_t)ks * 1536 * DM + nt * 128;
            u4 xr[2][2], wr[2][2];
            auto gload = [&](int k0, u4 (&xq)[2], u4 (&wq)[2]) {
#pragma unroll
                for (int i = 0; i < 2; ++i) { int c = tid + i * 512, r = c >> 3, kc = c & 7; xq[i] = *(const u4*)(Og + (size_t)(rb * 128 + r) * HD + ks * 1536 + k0 + kc * 8); }
#pragma unroll
                for (int i = 0; i < 2; ++i) { int c = tid + i * 512, r = c >> 4, nc = c & 15; wq[i] = *(const u4*)(Bw + (size_t)(k0 + r) * DM + nc * 8); }
            };
            auto swrite = [&](int buf, u4 (&xq)[2], u4 (&wq)[2]) {
                char* xs = smem + buf * (XT + OWT); char* wsm = xs + XT;
#pragma unroll
                for (int i = 0; i < 2; ++i) { int c = tid + i * 512, r = c >> 3, kc = c & 7; *(u4*)(xs + r * XS + kc * 16) = xq[i]; }
#pragma unroll
                for (int i = 0; i < 2; ++i) { int c = tid + i * 512, r = c >> 4, nc = c & 15; *(u4*)(wsm + r * OWSR + nc * 16) = wq[i]; }
            };
            f4 acc[2][4];
#pragma unroll
            for (int i = 0; i < 2; ++i)
#pragma unroll
                for (int jj = 0; jj < 4; ++jj) acc[i][jj] = (f4){0.f, 0.f, 0.f, 0.f};
            auto compute = [&](int buf) {
                const char* xs = smem + buf * (XT + OWT);
                const unsigned wsb = (unsigned)(size_t)(smem + buf * (XT + OWT) + XT);
#pragma unroll
                for (int k2 = 0; k2 < 2; ++k2) {
                    bf8 wf[2], xf[4];
#pragma unroll
                    for (int jj = 0; jj < 4; ++jj) xf[jj] = *(const bf8*)(xs + (wm * 64 + jj * 16 + li) * XS + (k2 * 32 + 8 * g) * 2);
                    tr_read_frags<2, 4 * OWSR>(wsb + (k2 * 32 + 8 * g + q) * OWSR + (wn * 32 + 4 * p) * 2, wf);
#pragma unroll
                    for (int i = 0; i < 2; ++i)
#pragma unroll
                        for (int jj = 0; jj < 4; ++jj) acc[i][jj] = mfma16(wf[i], xf[jj], acc[i][jj]);
                }
            };
            gload(0, xr[0], wr[0]);
            gload(BK, xr[1], wr[1]);
            swrite(0, xr[0], wr[0]);
            BAR();
            for (int kt = 0; kt < 24; kt += 2) {
                if (kt + 2 < 24) gload((kt + 2) * BK, xr[0], wr[0]);
                compute(0);
                swrite(1, xr[1], wr[1]);
                BAR();
                if (kt + 3 < 24) gload((kt + 3) * BK, xr[1], wr[1]);
                compute(1);
                if (kt + 2 < 24) swrite(0, xr[0], wr[0]);
                BAR();
            }
            float* part = (float*)(ws + WS_PART);
#pragma unroll
            for (int i = 0; i < 2; ++i)
#pragma unroll
                for (int jj = 0; jj < 4; ++jj) {
                    const int m = rb * 128 + wm * 64 + jj * 16 + li, n = nt * 128 + wn * 32 + i * 16 + 4 * g;
                    *(f4*)(part + ((size_t)ks * S_LEN + m) * DM + n) = acc[i][jj];
                }
            continue;
        }
        const int hh = it;
        if (tid == 0) {
            const unsigned* fl = (const unsigned*)(ws + WS_FLAG + (rb * 32 + hh) * 64);
            unsigned spins = 0;
            while (__hip_atomic_load(fl, __ATOMIC_RELAXED, __HIP_MEMORY_SCOPE_AGENT) < 2u ||
                   __hip_atomic_load(fl + 8, __ATOMIC_RELAXED, __HIP_MEMORY_SCOPE_AGENT) < 1u) {
                if (++spins > SPIN_MAX) { *tmo = 5; break; }
                __builtin_amdgcn_s_sleep(8);
            }
            const unsigned* ew = (const unsigned*)(ws + WS_E2 + (size_t)(rb * 4 + 3) * E2_STEP + (size_t)hh * 9216 + 9212);
            spins = 0;
            while (__hip_atomic_load(ew, __ATOMIC_RELAXED, __HIP_MEMORY_SCOPE_AGENT) == SENT) {
                if (++spins > SPIN_MAX) { *tmo = 7; break; }
                __builtin_amdgcn_s_sleep(48);
            }
        }
        __syncthreads();
        __amdgpu_buffer_rsrc_t rq = mkrsrc(Qx + (size_t)rb * 128 * HD, 128 * HD * 2);
        __amdgpu_buffer_rsrc_t rk = mkrsrc(Kx + (size_t)rb * 128 * HD, 128 * HD * 2);
        __amdgpu_buffer_rsrc_t rv = mkrsrc(Vx + (size_t)rb * 128 * HD, 128 * HD * 2);
        const int sl = wave >> 1, s = rb * 4 + sl;
        __amdgpu_buffer_rsrc_t re = mkrsrc(ws + WS_E2 + (size_t)s * E2_STEP + (size_t)hh * 9216, 9216);
        char* sv = smem + sl * (48 * 400);
        const int t2 = tid & 127;
        for (int c = t2; c < 32 * 24; c += 128) {
            int r = c / 24, cc = c % 24;
            *(u4*)(sv + r * 400 + cc * 16) = ld16_sc1(rv, ((sl * 32 + r) * HD + hh * DH + cc * 8) * 2);
        }
        for (int c = t2; c < 8 * 24; c += 128) *(u4*)(sv + (40 + c / 24) * 400 + (c % 24) * 16) = (u4){0u, 0u, 0u, 0u};
        bf8 km[6];
        {
            const int c0 = t2, c1 = t2 + 128 < 192 ? t2 + 128 : t2;
            const int o0 = ((24 + ((c0 % 24) >> 1)) * 128 + (c0 / 24) * 16 + (c0 & 1) * 8) * 2;
            const int o1 = ((24 + ((c1 % 24) >> 1)) * 128 + (c1 / 24) * 16 + (c1 & 1) * 8) * 2;
            const int ko = ((12 + (g >> 1)) * 128 + (li & 7) * 16 + (g & 1) * 8) * 2;
            unsigned spins = 0;
            u4 v0, v1;
            for (;;) {
                asm volatile("" ::: "memory");
                u4 kk4[6]; bool ok = true;
                v0 = ld16_sc1(re, o0); v1 = ld16_sc1(re, o1);
#pragma unroll
                for (int kk = 0; kk < 6; ++kk) kk4[kk] = ld16_sc1(re, ko + kk * 512);
#pragma unroll
                for (int kk = 0; kk < 6; ++kk) { ok &= ok4(kk4[kk]); km[kk] = __builtin_bit_cast(bf8, kk4[kk]); }
                if (__all(ok && ok4(v0) && ok4(v1))) break;
                if (++spins > SPIN_MAX) { *tmo = 6; break; }
                __builtin_amdgcn_s_sleep(32);
            }
            *(u4*)(sv + (32 + c0 / 24) * 400 + (c0 % 24) * 16) = v0;
            *(u4*)(sv + (32 + c1 / 24) * 400 + (c1 % 24) * 16) = v1;
        }
        const int row = (wave & 1) * 16 + li;
        f4 sc[3];
#pragma unroll
        for (int t = 0; t < 3; ++t) sc[t] = (f4){0.f, 0.f, 0.f, 0.f};
#pragma unroll
        for (int kk = 0; kk < 6; ++kk) {
            bf8 qf = __builtin_bit_cast(bf8, ld16_sc1(rq, ((sl * 32 + row) * HD + hh * DH + kk * 32 + 8 * g) * 2));
            bf8 k0 = __builtin_bit_cast(bf8, ld16_sc1(rk, ((sl * 32 + li) * HD + hh * DH + kk * 32 + 8 * g) * 2));
            bf8 k1 = __builtin_bit_cast(bf8, ld16_sc1(rk, ((sl * 32 + 16 + li) * HD + hh * DH + kk * 32 + 8 * g) * 2));
            sc[0] = mfma16(k0, qf, sc[0]);
            sc[1] = mfma16(k1, qf, sc[1]);
            sc[2] = mfma16(km[kk], qf, sc[2]);
        }
        float sv2[12];
#pragma unroll
        for (int t = 0; t < 3; ++t)
#pragma unroll
            for (int e = 0; e < 4; ++e) {
                float v = rbf(rbf(sc[t][e]) * scale);
                const int key = t * 16 + 4 * g + e;
                bool valid = t < 2 ? (key <= row) : (g < 2);
                sv2[t * 4 + e] = valid ? v : -INFINITY;
            }
        float mx = sv2[0];
#pragma unroll
        for (int e = 1; e < 12; ++e) mx = fmaxf(mx, sv2[e]);
        mx = xmax32(xmax16(mx));
        float sum = 0.f;
#pragma unroll
        for (int e = 0; e < 12; ++e) { sv2[e] = __expf(sv2[e] - mx); sum += sv2[e]; }
        sum = xsum32(xsum16(sum));
        const float inv = 1.0f / sum;
        u4 pb0 = {pack2(sv2[0] * inv, sv2[1] * inv), pack2(sv2[2] * inv, sv2[3] * inv), pack2(sv2[4] * inv, sv2[5] * inv), pack2(sv2[6] * inv, sv2[7] * inv)};
        u2 pb1 = {pack2(sv2[8] * inv, sv2[9] * inv), pack2(sv2[10] * inv, sv2[11] * inv)};
        const bf8 pf0 = __builtin_bit_cast(bf8, pb0);
        __syncthreads();
        const unsigned lb = (unsigned)(size_t)sv;
        __amdgpu_buffer_rsrc_t rog = mkrsrc(Og + (size_t)rb * 128 * HD, 128 * HD * 2);
#pragma unroll
        for (int dt = 0; dt < 12; ++dt) {
            unsigned a0 = lb + (4 * g + q) * 400 + (dt * 16 + 4 * p) * 2;
            bf8 vf0 = tr_read2(a0, a0 + 16 * 400);
            u2 vm = tr_read(a0 + 32 * 400);
            u4 vt = {vm.x, vm.y, 0u, 0u}, pt = {pb1.x, pb1.y, 0u, 0u};
            f4 a = mfma16(__builtin_bit_cast(bf8, vt), __builtin_bit_cast(bf8, pt), mfma16(vf0, pf0, (f4){0.f, 0.f, 0.f, 0.f}));
            u2 o = {pack2(a.x, a.y), pack2(a.z, a.w)};
            st8_sc1(rog, ((sl * 32 + row) * HD + hh * DH + dt * 16 + 4 * g) * 2, o);
        }
        asm volatile("s_waitcnt vmcnt(0)" ::: "memory");
        __syncthreads();
        if (tid == 0) __hip_atomic_fetch_add((unsigned*)(ws + WS_CNTA + rb * 64), 1u, __ATOMIC_RELAXED, __HIP_MEMORY_SCOPE_AGENT);
    }
}

__global__ void __launch_bounds__(512, 1) recur_kernel(const u16* __restrict__ mem0,
                                                       const u16* __restrict__ Wq, const u16* __restrict__ Wk, const u16* __restrict__ Wv,
                                                       const u16* __restrict__ Wo,
                                                       const u16* __restrict__ xn, u16* Qx, u16* Kx, u16* Vx,
                                                       char* __restrict__ ws) {
    extern __shared__ __attribute__((aligned(16))) char smem[];
    const int tid = threadIdx.x, lane = tid & 63, wave = __builtin_amdgcn_readfirstlane(tid >> 6);
    if (blockIdx.x >= 128) { qkv_gemm_role(smem, blockIdx.x - 128, xn, Wq, Wk, Wv, Wo, Qx, Kx, Vx, (u16*)(ws + WS_O), ws);
        return; }
    const int b = blockIdx.x;
    const int xcd = b & 7, idx = b >> 3;
    const int h = xcd * 4 + (idx >> 2), j = idx & 3;
    const int g = lane >> 4, li = lane & 15, q = li >> 2, p = li & 3;
    const unsigned lbase = (unsigned)(size_t)smem;
    int* tmo = (int*)(ws + WS_TMO);
    const float scale = 0.07216878364870322f;
    int dly1 = DLY1, dly2 = DLY2, dly3 = DLY3;

    auto kv_dma = [&](int s) {
        const int bufo = (s & 1) * KV_BUF;
        if ((s & 3) == 0) {
            const unsigned* fl = (const unsigned*)(ws + WS_FLAG + ((s >> 2) * 32 + h) * 64);
            unsigned spins = 0;
            while (__hip_atomic_load(fl, __ATOMIC_RELAXED, __HIP_MEMORY_SCOPE_AGENT) < 2u) {
                if (++spins > SPIN_MAX) { *tmo = 4; break; }
                __builtin_amdgcn_s_sleep(4);
            }
        }
#pragma unroll 1
        for (int i = 0; i < 12; ++i) {
            const int L = i * 64 + lane, r = L / 24, cp = L % 24, c = (cp & 24) | ((cp ^ r) & 7);
            const size_t goff = (size_t)(s * SEG + r) * HD + h * DH + c * 8;
            __builtin_amdgcn_global_load_lds((const unsigned*)(Kx + goff), (unsigned*)(smem + L_KX + bufo + i * 1024), 16, 0, 16);
            __builtin_amdgcn_global_load_lds((const unsigned*)(Vx + goff), (unsigned*)(smem + L_VX + bufo + i * 1024), 16, 0, 16);
        }
    };

    auto tail = [&](int s) {
        __amdgpu_buffer_rsrc_t r3 = mkrsrc(ws + WS_E3 + (size_t)(s & (E3_RING - 1)) * E3_STEP + (size_t)j * (32 * 32 * 40 * 4), 32 * 32 * 40 * 4);
        if (tid < 320) {
            const int e = tid * 4;
            f4 a = *(const f4*)(smem + L_RED2 + e * 4);
#pragma unroll
            for (int w2 = 1; w2 < 4; ++w2) a += *(const f4*)(smem + L_RED2 + (w2 * 1280 + e) * 4);
            const int dst = e / 40, pos = e % 40;
            const u4 au = __builtin_bit_cast(u4, a);
            u4 o = {nosent(au.x), nosent(au.y), nosent(au.z), nosent(au.w)};
            st16_sc1(r3, ((dst * 32 + h) * 40 + pos) * 4, o);
        }
        STW(5);
        if (wave >= 3) {
            const int pw = wave - 3, src = lane & 31, part = 2 * pw + (lane >> 5);
            u4 v0;
            unsigned spins = 0;
            DELAY(dly3);
            for (;;) {
                asm volatile("" ::: "memory");
                v0 = ld16_sc1(r3, ((h * 32 + src) * 40 + part * 4) * 4);
                if (__all(ok4(v0))) break;
                if (++spins > SPIN_MAX) { *tmo = 3; break; }
            }
            ADAPT(dly3, spins);
            STW(6);
            f4 a = __builtin_bit_cast(f4, v0);
            a.x = sum32(a.x); a.y = sum32(a.y); a.z = sum32(a.z); a.w = sum32(a.w);
            __amdgpu_buffer_rsrc_t r1 = mkrsrc(ws + WS_E1 + (size_t)(s + 1) * E1_STEP, (unsigned)E1_STEP);
            const int row = h >> 2, col = 160 * j + (h & 3) * 40 + 8 * pw;
            unsigned w0 = nosent(pack2(a.x, a.y)), w1 = nosent(pack2(a.z, a.w));
            auto s0 = __builtin_amdgcn_permlane32_swap(w0, w0, false, false);
            auto s1 = __builtin_amdgcn_permlane32_swap(w1, w1, false, false);
            u4 o = {(unsigned)s0[0], (unsigned)s1[0], (unsigned)s0[1], (unsigned)s1[1]};
            if (lane < E1_REP) st16_sc1(r1, lane * (int)E1_ONE + (((col >> 5) * 4 + ((col >> 3) & 3)) * 8 + row) * 16, o);
            st16_sc1(r3, ((h * 32 + src) * 40 + part * 4) * 4, (u4){SENT, SENT, SENT, SENT});
            STW(7);
        }
    };

    if (wave < 4) {
        const int kq = wave;
        bf8 wqkv[9][5];
        {
            constexpr int SS = 320;
#pragma unroll
            for (int kc = 0; kc < 4; ++kc) {
                BAR();
                BAR();
                if (kq == kc) {
#pragma unroll
                    for (int t = 0; t < 9; ++t)
#pragma unroll
                        for (int kk = 0; kk < 5; ++kk) {
                            unsigned a0 = lbase + L_STAGE + (kk * 32 + 8 * g + q) * SS + (t * 16 + 4 * p) * 2;
                            wqkv[t][kk] = tr_read2(a0, a0 + 4 * SS);
                        }
                }
            }
            BAR();
            BAR();
        }
        if (wave == 0) kv_dma(0);
        for (int s = 0; s < NSEG; ++s) {
            STW(0);
            bf8 mf[5];
            const int moff = ((li & 7) * DM + (5 * kq) * 32 + 8 * g) * 2;
            const int foff = ((5 * kq * 4 + g) * 8 + (li & 7)) * 16;
            if (s == 0) {
#pragma unroll
                for (int kk = 0; kk < 5; ++kk) mf[kk] = *(const bf8*)((const char*)mem0 + moff + kk * 64);
                asm volatile("s_waitcnt vmcnt(0)" ::: "memory");
            } else {
                __amdgpu_buffer_rsrc_t r = mkrsrc(ws + WS_E1 + (size_t)s * E1_STEP + (size_t)xcd * E1_ONE, (unsigned)E1_ONE);
                unsigned spins = 0;
                DELAY(dly1);
                for (;;) {
                    asm volatile("" ::: "memory");
                    u4 v[5]; bool ok = true;
#pragma unroll
                    for (int kk = 0; kk < 5; ++kk) v[kk] = ld16_sc1(r, foff + kk * 512);
#pragma unroll
                    for (int kk = 0; kk < 5; ++kk) { ok &= ok4(v[kk]); mf[kk] = __builtin_bit_cast(bf8, v[kk]); }
                    if (__all(ok)) break;
                    if (++spins > SPIN_MAX) { *tmo = 1; break; }
                }
                ADAPT(dly1, spins);
            }
            STW(1);
#pragma unroll
            for (int tg = 0; tg < 2; ++tg) {
                constexpr int NT0 = 5;
                f4 acc[NT0];
#pragma unroll
                for (int t = 0; t < NT0; ++t) acc[t] = (f4){0.f, 0.f, 0.f, 0.f};
#pragma unroll
                for (int kk = 0; kk < 5; ++kk)
#pragma unroll
                    for (int t = 0; t < NT0; ++t) { const int tt = tg * 5 + t; if (tt < 9) acc[t] = mfma16(wqkv[tt < 9 ? tt : 8][kk], mf[kk], acc[t]); }
                if (li < 8) {
#pragma unroll
                    for (int t = 0; t < NT0; ++t) { const int tt = tg * 5 + t; if (tt < 9) *(f4*)(smem + L_RED + (((kq * 9 + tt) * 8 + li) * 16 + 4 * g) * 4) = acc[t]; }
                }
            }
            STW(2);
            BAR();
            STW(3);
            __amdgpu_buffer_rsrc_t r2 = mkrsrc(ws + WS_E2 + (size_t)s * E2_STEP + (size_t)h * (3 * 8 * 192 * 2), 3 * 8 * 192 * 2);
            if (tid < 144) {
                f4 a0 = *(const f4*)(smem + L_RED + tid * 32), a1 = *(const f4*)(smem + L_RED + tid * 32 + 16);
#pragma unroll
                for (int k2 = 1; k2 < 4; ++k2) {
                    a0 += *(const f4*)(smem + L_RED + k2 * 4608 + tid * 32);
                    a1 += *(const f4*)(smem + L_RED + k2 * 4608 + tid * 32 + 16);
                }
                u4 o = {nosent(pack2(a0.x, a0.y)), nosent(pack2(a0.z, a0.w)), nosent(pack2(a1.x, a1.y)), nosent(pack2(a1.z, a1.w))};
                st16_sc1(r2, j * 2304 + tid * 16, o);
            }
            STW(4);
            if (s == NSEG - 1) break;
            BAR();
            if (wave == 0) kv_dma(s + 1);
            BAR();
            tail(s);
            BAR();
        }
        STDUMP();
    } else {
        const int aw = wave - 4;
        bf8 wo0[10]; u2 wo1[10];
        {
            const int t2 = tid - 256;
            u4 stg[15];
#pragma unroll
            for (int kc = 0; kc <= 5; ++kc) {
                if (kc >= 1) {
                    if (kc <= 4) BAR();
#pragma unroll
                    for (int i = 0; i < 15; ++i) {
                        const int c = t2 + i * 256;
                        if (kc - 1 < 4) { if (c < 160 * 18) *(u4*)(smem + L_STAGE + (c / 18) * 320 + (c % 18) * 16) = stg[i]; }
                        else if (c < 192 * 20) *(u4*)(smem + L_KX + (c / 20) * 352 + (c % 20) * 16) = stg[i];
                    }
                }
                if (kc <= 4) {
#pragma unroll
                    for (int i = 0; i < 15; ++i) {
                        const int c = t2 + i * 256;
                        if (kc < 4) {
                            if (c < 160 * 18) {
                                int r = c / 18, cc = c % 18, gt = 9 * j + (cc >> 1), m = gt / 12, col = (gt % 12) * 16 + (cc & 1) * 8;
                                const u16* W = m == 0 ? Wq : (m == 1 ? Wk : Wv);
                                stg[i] = *(const u4*)(W + (size_t)(160 * kc + r) * HD + h * DH + col);
                            }
                        } else if (c < 192 * 20) {
                            int r = c / 20, cc = c % 20;
                            stg[i] = *(const u4*)(Wo + (size_t)(h * DH + r) * DM + 160 * j + cc * 8);
                        }
                    }
                }
                if (kc >= 1) BAR();
            }
#pragma unroll
            for (int ct = 0; ct < 10; ++ct) {
                unsigned a0 = lbase + L_KX + (48 * aw + 4 * g + q) * 352 + (ct * 16 + 4 * p) * 2;
                wo0[ct] = tr_read2(a0, a0 + 16 * 352);
                wo1[ct] = tr_read(a0 + 32 * 352);
            }
            BAR();
        }
        for (int s = 0; s < NSEG; ++s) {
            BAR();
            if (s == NSEG - 1) break;
            const int bufo = (s & 1) * KV_BUF;
            STW(0);
            bf8 kx[2][6], vx[3];
#pragma unroll
            for (int kk = 0; kk < 6; ++kk) {
                const int kc = kk * 4 + g, kcs = ((kc & 24) | ((kc ^ li) & 7)) * 16;
                kx[0][kk] = *(const bf8*)(smem + L_KX + bufo + li * 384 + kcs);
                kx[1][kk] = *(const bf8*)(smem + L_KX + bufo + (16 + li) * 384 + kcs);
            }
#pragma unroll
            for (int dt = 0; dt < 3; ++dt) {
                const int d0 = (3 * aw + dt) * 16;
                const int vr = 4 * g + q, vc = (d0 >> 3) + (p >> 1), vcs = ((vc & 24) | ((vc ^ vr) & 7)) * 16 + (p & 1) * 8;
                unsigned a0 = lbase + L_VX + bufo + vr * 384 + vcs;
                vx[dt] = tr_read2(a0, a0 + 16 * 384);
            }
            STW(1);
            __amdgpu_buffer_rsrc_t r2 = mkrsrc(ws + WS_E2 + (size_t)s * E2_STEP + (size_t)h * (3 * 8 * 192 * 2), 3 * 8 * 192 * 2);
            {
                const int t = tid - 256;
                int cs[3] = {t, t + 256, t + 512 < 576 ? t + 512 : t};
                u4 v[3];
                unsigned spins = 0;
                DELAY(dly2);
                for (;;) {
                    asm volatile("" ::: "memory");
#pragma unroll
                    for (int i = 0; i < 3; ++i) v[i] = ld16_sc1(r2, cs[i] * 16);
                    if (__all(ok4(v[0]) && ok4(v[1]) && ok4(v[2]))) break;
                    if (++spins > SPIN_MAX) { *tmo = 2; break; }
                }
                ADAPT(dly2, spins);
#pragma unroll
                for (int i = 0; i < 3; ++i) {
                    const int gt = cs[i] >> 4, w = cs[i] & 15;
                    *(u4*)(smem + L_QK + ((gt / 12) * 8 + (w >> 1)) * 400 + ((gt % 12) * 16 + (w & 1) * 8) * 2) = v[i];
                }
            }
            BAR();
            bf8 qf[6], km[6]; u2 vm[3];
#pragma unroll
            for (int kk = 0; kk < 6; ++kk) {
                qf[kk] = *(const bf8*)(smem + L_QK + (li & 7) * 400 + (kk * 32 + 8 * g) * 2);
                km[kk] = *(const bf8*)(smem + L_QK + (8 + (li & 7)) * 400 + (kk * 32 + 8 * g) * 2);
            }
#pragma unroll
            for (int dt = 0; dt < 3; ++dt) vm[dt] = tr_read(lbase + L_QK + (16 + ((4 * g + q) & 7)) * 400 + ((3 * aw + dt) * 16 + 4 * p) * 2);
            STW(2);
            f4 sc[3];
#pragma unroll
            for (int t = 0; t < 3; ++t) sc[t] = (f4){0.f, 0.f, 0.f, 0.f};
#pragma unroll
            for (int kk = 0; kk < 6; ++kk) {
                sc[0] = mfma16(kx[0][kk], qf[kk], sc[0]);
                sc[1] = mfma16(kx[1][kk], qf[kk], sc[1]);
                sc[2] = mfma16(km[kk], qf[kk], sc[2]);
            }
            float sv[12];
#pragma unroll
            for (int t = 0; t < 3; ++t)
#pragma unroll
                for (int e = 0; e < 4; ++e) sv[t * 4 + e] = rbf(rbf(sc[t][e]) * scale);
            if (g >= 2) {
#pragma unroll
                for (int e = 0; e < 4; ++e) sv[8 + e] = -INFINITY;
            }
            float mx = sv[0];
#pragma unroll
            for (int e = 1; e < 12; ++e) mx = fmaxf(mx, sv[e]);
            mx = xmax32(xmax16(mx));
            float sum = 0.f;
#pragma unroll
            for (int e = 0; e < 12; ++e) { sv[e] = __expf(sv[e] - mx); sum += sv[e]; }
            sum = xsum32(xsum16(sum));
            const float inv = 1.0f / sum;
            u4 pb0 = {pack2(sv[0] * inv, sv[1] * inv), pack2(sv[2] * inv, sv[3] * inv), pack2(sv[4] * inv, sv[5] * inv), pack2(sv[6] * inv, sv[7] * inv)};
            u2 pb1 = {pack2(sv[8] * inv, sv[9] * inv), pack2(sv[10] * inv, sv[11] * inv)};
            const bf8 pf0 = __builtin_bit_cast(bf8, pb0);
            f4 oa[3];
#pragma unroll
            for (int dt = 0; dt < 3; ++dt) {
                f4 a = mfma16(vx[dt], pf0, (f4){0.f, 0.f, 0.f, 0.f});
                oa[dt] = mfma16k16(vm[dt], pb1, a);
            }
            u4 ob0 = {pack2(oa[0].x, oa[0].y), pack2(oa[0].z, oa[0].w), pack2(oa[1].x, oa[1].y), pack2(oa[1].z, oa[1].w)};
            u2 ob1 = {pack2(oa[2].x, oa[2].y), pack2(oa[2].z, oa[2].w)};
            const bf8 of0 = __builtin_bit_cast(bf8, ob0);
            f4 pa[10];
#pragma unroll
            for (int ct = 0; ct < 10; ++ct) pa[ct] = mfma16k16(wo1[ct], ob1, mfma16(wo0[ct], of0, (f4){0.f, 0.f, 0.f, 0.f}));
            if (li < 8) {
#pragma unroll
                for (int ct = 0; ct < 10; ++ct) *(f4*)(smem + L_RED2 + ((aw * 8 + li) * 160 + ct * 16 + 4 * g) * 4) = pa[ct];
            }
            STW(3);
            BAR();
            STW(4);
            tail(s);
            BAR();
        }
        STDUMP();
    }
}

extern "C" void kernel_launch(void* const* d_in, const int* in_sizes, int n_in,
                              void* d_out, int out_size, void* d_ws, size_t ws_size,
                              hipStream_t stream) {
    const u16* x    = (const u16*)d_in[0];
    const u16* mem0 = (const u16*)d_in[1];
    const u16* Wq   = (const u16*)d_in[2];
    const u16* Wk   = (const u16*)d_in[3];
    const u16* Wv   = (const u16*)d_in[4];
    const u16* Wo   = (const u16*)d_in[5];
    const u16* w1   = (const u16*)d_in[8];
    const u16* b1   = (const u16*)d_in[9];
    const u16* w2   = (const u16*)d_in[10];
    const u16* b2   = (const u16*)d_in[11];
    u16* out = (u16*)d_out;
    char* ws = (char*)d_ws;
    u16* xn = (u16*)(ws + WS_XN);
    u16* Qx = (u16*)(ws + WS_Q);
    u16* Kx = (u16*)(ws + WS_K);
    u16* Vx = (u16*)(ws + WS_V);
    u16* O  = (u16*)(ws + WS_O);
    u16* H  = (u16*)(ws + WS_H);
    u16* Y  = (u16*)(ws + WS_Y);
    u16* HID = (u16*)(ws + WS_HID);

    prep_kernel<<<1024, 256, 0, stream>>>(x, xn, ws);
    (void)hipFuncSetAttribute((const void*)recur_kernel, hipFuncAttributeMaxDynamicSharedMemorySize, L_TOTAL);
    recur_kernel<<<256, 512, L_TOTAL, stream>>>(mem0, Wq, Wk, Wv, Wo, xn, Qx, Kx, Vx, ws);
    float* PART = (float*)(ws + WS_PART);
    red_ln_kernel<4><<<S_LEN / 4, 256, 0, stream>>>(PART, x, H, Y);
#define UBM 64
#define UBN 64
#define DBM 64
#define DBN 64
    gemm_kernel<UBM, UBN, 2><<<dim3(FF / UBN, S_LEN / UBM), 256, 0, stream>>>(Y, DM, w1, w1, w1, FF, HID, HID, HID, FF, DM, 1, b1, nullptr, nullptr);
    gemm_kernel<DBM, DBN, 3><<<dim3(DM / DBN, S_LEN / DBM), 256, 0, stream>>>(HID, FF, w2, w2, w2, DM, out, out, out, DM, FF, 1, b2, H, nullptr);
}
```
